```python
import torch

def _softmax(x, axis=-1):
    return torch.softmax(x, dim=axis)

def _gelu(x):
    return torch.nn.functional.gelu(x, approximate='tanh')

def _silu(x):
    return torch.nn.functional.silu(x)

def _sigmoid(x):
    return torch.sigmoid(x)

def _layernorm(x, axis=-1, eps=1e-05):
    m = x.mean(dim=axis, keepdim=True)
    v = x.var(dim=axis, keepdim=True, unbiased=False)
    return (x - m) / torch.sqrt(v + eps)

def _scatter_add(tgt, idx, src, axis=0):
    return tgt.clone().index_add_(axis, idx, src)

def _segment_sum(src, seg, n):
    out = torch.zeros((n,) + src.shape[1:], dtype=src.dtype)
    return out.index_add_(0, seg, src)

def _pad(x, pw):
    flat = tuple((v for pair in reversed(pw) for v in pair))
    return torch.nn.functional.pad(x, flat)
kernel_name = 'standalone__memory_aug_recurrent_mem_tr__prompt'

def setup_inputs():

    _w = lambda a: (lambda t: t.to(torch.bfloat16) if t.is_floating_point() else t)(torch.as_tensor(a))
    B, S, Dm = (1, 2048, 640)
    H, Dh = (32, 128)
    M, seg = (8, 32)
    seg = min(seg, S)
    S = S // seg * seg
    return {'x': _w((lambda *s: torch.randn(*s, dtype=torch.bfloat16) * 0.05)(B, S, Dm)), 'mem0': _w((lambda *s: torch.randn(*s, dtype=torch.bfloat16) * 0.4)(B, M, Dm)), 'Wq': _w((lambda *s: torch.randn(*s, dtype=torch.bfloat16) * 6.4)(Dm, H, Dh) * Dm ** (-0.5)), 'Wk': _w((lambda *s: torch.randn(*s, dtype=torch.bfloat16) * 6.4)(Dm, H, Dh) * Dm ** (-0.5)), 'Wv': _w((lambda *s: torch.randn(*s, dtype=torch.bfloat16) * 0.4)(Dm, H, Dh) * Dm ** (-0.5)), 'Wo': _w((lambda *s: torch.randn(*s, dtype=torch.bfloat16) * 0.4)(H, Dh, Dm) * (H * Dh) ** (-0.5)), 'seg_len': seg}

def reference(x, mem0, Wq, Wk, Wv, Wo, seg_len):
    B, S, Dm = x.shape
    H, Dh = (Wq.shape[1], Wq.shape[2])
    M = mem0.shape[1]
    scale = Dh ** (-0.5)
    n_seg = S // seg_len
    L = M + seg_len
    idx = torch.arange(L)
    mask = (idx[:, None] < M) | (idx[:, None] >= idx[None, :])
    mem = mem0
    outs = []
    for s in range(n_seg):
        xs = x[:, s * seg_len:(s + 1) * seg_len, :].clone()
        h = torch.concatenate([mem, xs], axis=1)
        q = torch.einsum('btd,dhe->bhte', h, Wq)
        k = torch.einsum('btd,dhe->bhte', h, Wk)
        v = torch.einsum('btd,dhe->bhte', h, Wv)
        scores = torch.einsum('bhqe,bhke->bhqk', q, k) * scale
        scores = torch.where(mask[None, None], scores, -1000000000.0)
        p = _softmax(scores, axis=-1)
        o = torch.einsum('bhqk,bhke->bhqe', p, v)
        o = torch.einsum('bhte,hed->btd', o, Wo)
        mem = o[:, :M, :].clone()
        outs.append(o[:, M:, :])
    return torch.concatenate(outs, axis=1)
```

```cpp
#include <hip/hip_runtime.h>
#include <hip/hip_bf16.h>
typedef unsigned short u16;
static __device__ __forceinline__ float bf2f(u16 v){ return __uint_as_float(((unsigned)v)<<16); }
static __device__ __forceinline__ u16 f2bf(float f){ __hip_bfloat16 b = __float2bfloat16(f); return *(u16*)&b; }
constexpr int DM=640,H=32,DH=128,M=8,SEG=32,L=40,HD=H*DH;
__global__ void __launch_bounds__(256) k_qkv(const u16* mem,const u16* xs,const u16* Wq,const u16* Wk,const u16* Wv,u16* qkv){
  __shared__ float hs[L][DM];
  for(int i=threadIdx.x;i<L*DM;i+=256){int r=i/DM,c=i%DM; hs[r][c]=bf2f(r<M?mem[r*DM+c]:xs[(r-M)*DM+c]);}
  __syncthreads();
  int col=blockIdx.x*256+threadIdx.x; int w=col/HD, c=col%HD;
  const u16* W = w==0?Wq:(w==1?Wk:Wv);
  float acc[L];
  #pragma unroll
  for(int r=0;r<L;r++)acc[r]=0.f;
  for(int d=0;d<DM;d++){ float wv=bf2f(W[(size_t)d*HD+c]);
    #pragma unroll
    for(int r=0;r<L;r++)acc[r]+=hs[r][d]*wv; }
  #pragma unroll
  for(int r=0;r<L;r++) qkv[((size_t)w*L+r)*HD+c]=f2bf(acc[r]);
}
__global__ void __launch_bounds__(128) k_attn(const u16* qkv,u16* o){
  __shared__ float p[L][L];
  int h=blockIdx.x;
  const u16* q=qkv+h*DH; const u16* k=qkv+(size_t)L*HD+h*DH; const u16* v=qkv+(size_t)2*L*HD+h*DH;
  for(int i=threadIdx.x;i<L*L;i+=128){int r=i/L,c=i%L; float s=0; for(int e=0;e<DH;e++) s+=bf2f(q[r*HD+e])*bf2f(k[c*HD+e]);
    s=bf2f(f2bf(s)); s=bf2f(f2bf(s*0.08838834764831845f)); if(!(r<M||r>=c)) s=-1e9f; p[r][c]=s;}
  __syncthreads();
  if(threadIdx.x<L){int r=threadIdx.x; float m=-INFINITY; for(int c=0;c<L;c++)m=fmaxf(m,p[r][c]); float l=0; for(int c=0;c<L;c++){float e=__expf(p[r][c]-m);p[r][c]=e;l+=e;} for(int c=0;c<L;c++)p[r][c]=bf2f(f2bf(p[r][c]/l));}
  __syncthreads();
  int e=threadIdx.x;
  for(int r=0;r<L;r++){float a=0; for(int c=0;c<L;c++)a+=p[r][c]*bf2f(v[c*HD+e]); o[r*HD+h*DH+e]=f2bf(a);}
}
__global__ void k_out(const u16* o,const u16* Wo,u16* memn,u16* out){
  int r=blockIdx.x; int d=blockIdx.y*64+threadIdx.x; float a=0;
  for(int i=0;i<HD;i++) a+=bf2f(o[r*HD+i])*bf2f(Wo[(size_t)i*DM+d]);
  if(r<M) memn[r*DM+d]=f2bf(a); else out[(r-M)*DM+d]=f2bf(a);
}
extern "C" void kernel_launch(void* const* d_in, const int* in_sizes, int n_in, void* d_out, int out_size, void* d_ws, size_t ws_size, hipStream_t stream) {
  const u16* x=(const u16*)d_in[0]; const u16* mem0=(const u16*)d_in[1];
  const u16* Wq=(const u16*)d_in[2]; const u16* Wk=(const u16*)d_in[3]; const u16* Wv=(const u16*)d_in[4]; const u16* Wo=(const u16*)d_in[5];
  u16* out=(u16*)d_out; u16* ws=(u16*)d_ws;
  u16* qkv=ws; u16* o=ws+3*L*HD; u16* mems=o+L*HD;
  int S=in_sizes[0]/DM; int nseg=S/SEG;
  for(int s=0;s<nseg;s++){
    const u16* mem = s==0?mem0:mems+(size_t)(s&1)*M*DM;
    k_qkv<<<3*HD/256,256,0,stream>>>(mem,x+(size_t)s*SEG*DM,Wq,Wk,Wv,qkv);
    k_attn<<<H,128,0,stream>>>(qkv,o);
    k_out<<<dim3(L,DM/64),64,0,stream>>>(o,Wo,mems+(size_t)((s+1)&1)*M*DM,out+(size_t)s*SEG*DM);
  }
}
```

```cpp
#include <hip/hip_runtime.h>
#include <hip/hip_bf16.h>
#include <utility>

typedef unsigned u32;
typedef unsigned long long u64;
typedef unsigned short u16;
typedef __attribute__((ext_vector_type(2))) __bf16 bf16x2;
typedef __attribute__((ext_vector_type(8))) __bf16 bf16x8;
typedef __attribute__((ext_vector_type(16))) __bf16 bf16x16;
typedef __attribute__((ext_vector_type(4))) float f32x4;
typedef __attribute__((ext_vector_type(4))) short s16x4;
typedef __attribute__((ext_vector_type(2))) u32 v2u;
typedef __attribute__((ext_vector_type(4))) u32 v4u;
typedef __attribute__((ext_vector_type(8))) u32 v8u;
typedef __amdgpu_buffer_rsrc_t rsrc_t;
#define DI __device__ __forceinline__
#define LAS __attribute__((address_space(3)))

constexpr int NSEG = 64;
constexpr u32 SENT = 0xFFFFFFFFu;
constexpr int SC1 = 16;
constexpr int F_TICK = 0;
constexpr int F_SPARE = 512;
constexpr int F_PAR = 544;
constexpr int F_ERR = 576;
constexpr int F_QK = 1024;
constexpr int F_VU = 1280;
constexpr int F_A = 1536;
constexpr int F_C = 1600;
constexpr int F_DBG = 4096;
constexpr u32 FLAG_BYTES = 65536 * 4;
constexpr u32 O_KX = 0;
constexpr u32 O_QX = O_KX + 64u * 32 * 8192;
constexpr u32 O_U = O_QX + 64u * 32 * 8192;
constexpr u32 O_Y2 = O_U + 64u * 80 * 16384;
constexpr u32 O_PU = O_Y2 + 64u * 80 * 4096;
constexpr u32 O_PM = O_PU + 64u * 65536;
constexpr u32 BIG_BYTES = O_PM + 64u * 16384;
constexpr u32 HOT_BYTES = 4u << 20;
constexpr u32 LDS_BYTES = 140 * 1024;

template <class F, int... I> DI void sfor_(F&& f, std::integer_sequence<int, I...>) { (f(std::integral_constant<int, I>{}), ...); }
template <int N, class F> DI void sfor(F&& f) { sfor_(f, std::make_integer_sequence<int, N>{}); }
#define CI(I) decltype(I)::value

DI u32 rfl(u32 x) { u32 r; asm volatile("s_nop 1\n\tv_readfirstlane_b32 %0, %1\n\ts_nop 4" : "=s"(r) : "v"(x)); return r; }
DI u32 pk(float a, float b) { bf16x2 v = {(__bf16)a, (__bf16)b}; return __builtin_bit_cast(u32, v); }
DI u32 canon(u32 x) { return x < 0xFFFFFFFEu ? x : 0xFFFFFFFEu; }
DI float bfr(float x) { return (float)(__bf16)x; }
DI u32 umax3(u32 a, u32 b, u32 c) { u32 m = a > b ? a : b; return m > c ? m : c; }
DI rsrc_t mkrs(const void* p, u32 bytes) { return __builtin_amdgcn_make_buffer_rsrc((void*)p, 0, bytes, 0x00020000); }
DI v4u ld16(rsrc_t r, u32 vo, u32 so) { return __builtin_bit_cast(v4u, __builtin_amdgcn_raw_buffer_load_b128(r, vo, so, SC1)); }
DI u32 ld4(rsrc_t r, u32 vo, u32 so) { return __builtin_amdgcn_raw_buffer_load_b32(r, vo, so, SC1); }
DI void st16(v4u v, rsrc_t r, u32 vo, u32 so) { __builtin_amdgcn_raw_buffer_store_b128(v, r, vo, so, SC1); }
DI void st8(v2u v, rsrc_t r, u32 vo, u32 so) { __builtin_amdgcn_raw_buffer_store_b64(v, r, vo, so, SC1); }
DI void st4(u32 v, rsrc_t r, u32 vo, u32 so) { __builtin_amdgcn_raw_buffer_store_b32(v, r, vo, so, SC1); }
template <int N> DI void wait_vm() { asm volatile("s_waitcnt vmcnt(%0)" ::"i"(N) : "memory"); }
DI void wait_lgkm() { asm volatile("s_waitcnt lgkmcnt(0)" ::: "memory"); }
DI void bar() { asm volatile("s_waitcnt lgkmcnt(0)\n\ts_barrier" ::: "memory"); }
DI void fence_c() { asm volatile("" ::: "memory"); }
DI void dma16(u32 lds, u32 vo, rsrc_t r, u32 so) {
    u32 keep;
    asm volatile("s_mov_b32 %0, m0\n\ts_mov_b32 m0, %1\n\ts_nop 4\n\tbuffer_load_dwordx4 %2, %3, %4 offen lds\n\ts_mov_b32 m0, %0"
                 : "=&s"(keep) : "s"(lds), "v"(vo), "s"(r), "s"(so) : "memory");
}
DI void dma16c(u32 lds, u32 vo, rsrc_t r, u32 so) {
    u32 keep;
    asm volatile("s_mov_b32 %0, m0\n\ts_mov_b32 m0, %1\n\ts_nop 4\n\tbuffer_load_dwordx4 %2, %3, %4 offen sc1 lds\n\ts_mov_b32 m0, %0"
                 : "=&s"(keep) : "s"(lds), "v"(vo), "s"(r), "s"(so) : "memory");
}
DI v2u trd(u32 a) { return __builtin_bit_cast(v2u, __builtin_amdgcn_ds_read_tr16_b64_v4i16((LAS s16x4*)a)); }
DI v4u lr16(u32 a) { return *(LAS v4u*)a; }
DI void lw16(u32 a, v4u v) { *(LAS v4u*)a = v; }
DI void lw8(u32 a, v2u v) { *(LAS v2u*)a = v; }
DI void lw4(u32 a, u32 v) { *(LAS u32*)a = v; }
DI f32x4 mfma(v4u a, v4u b, f32x4 c) { return __builtin_amdgcn_mfma_f32_16x16x32_bf16(__builtin_bit_cast(bf16x8, a), __builtin_bit_cast(bf16x8, b), c, 0, 0, 0); }
DI f32x4 smf(v4u a, v8u b, f32x4 c, u32 idx) { return __builtin_amdgcn_smfmac_f32_16x16x64_bf16(__builtin_bit_cast(bf16x8, a), __builtin_bit_cast(bf16x16, b), c, (int)idx, 0, 0); }
DI float f_(u32 x) { return __uint_as_float(x); }
DI u32 u_(float x) { return __float_as_uint(x); }
DI float ror8(float x) { return f_((u32)__builtin_amdgcn_update_dpp(0, (int)u_(x), 0x128, 0xf, 0xf, false)); }
DI u32 ror8u(u32 x) { return (u32)__builtin_amdgcn_update_dpp(0, (int)x, 0x128, 0xf, 0xf, false); }
DI float halfadd(float x, float y) { auto r = __builtin_amdgcn_permlane32_swap(u_(x), u_(y), false, false); return f_(r[0]) + f_(r[1]); }
DI float max16(float x) { auto r = __builtin_amdgcn_permlane16_swap(u_(x), u_(x), false, false); return fmaxf(f_(r[0]), f_(r[1])); }
DI float max32(float x) { auto r = __builtin_amdgcn_permlane32_swap(u_(x), u_(x), false, false); return fmaxf(f_(r[0]), f_(r[1])); }
DI float sum16(float x) { auto r = __builtin_amdgcn_permlane16_swap(u_(x), u_(x), false, false); return f_(r[0]) + f_(r[1]); }
DI float sum32(float x) { auto r = __builtin_amdgcn_permlane32_swap(u_(x), u_(x), false, false); return f_(r[0]) + f_(r[1]); }
DI u64 rt() { return __builtin_amdgcn_s_memrealtime(); }
DI u32 mt32() { return (u32)__builtin_amdgcn_s_memtime(); }

constexpr u32 SPIN_CAP = 1u << 18;
struct Ctx {
    rsrc_t hot, big, flg;
    u32 lane, wave, par0, par1;
    u32 dead;
};
DI bool spin(Ctx& c, u32& n) {
    if (c.dead) return true;
    if (++n > SPIN_CAP) { c.dead = 1; st4(1u, c.flg, F_ERR * 4, 0); return true; }
    return false;
}
DI void delay(u32 t0, u32 cyc) { for (u32 i = 0; i < 4096; ++i) { if (mt32() - t0 >= cyc) break; } }
DI u32 mem_slot(const Ctx& c, u32 sg) { return (2 * sg) * 16384u + c.par0 * 8192u; }
DI u32 p_slot(const Ctx& c, u32 sg) { return (6 + 3 * sg) * 16384u + c.par0 * 8192u; }
DI u32 y_slot(const Ctx& c, u32 sg) { return sg == 2 ? 128u * 16384u + c.par1 * 8192u : (15 + 40 * sg) * 16384u + c.par0 * 8192u; }
constexpr u32 kb(int i) { return (u32)(i >> 3) * 16384u + (u32)(i & 7) * 1024u; }

#define STAMP(slot, s, k) do {} while (0)
#define STAMPV(slot, s, k, v) do {} while (0)
#define DLY_A 0
#define DLY_B 0

template <class ISSUE, class SINK> DI void load_weights(const Ctx& c, ISSUE issue, SINK sink) {
    constexpr int NB = 80, RING = 32;
    const u32 ring = c.wave * (RING * 1024);
    const u32 gb = c.lane >> 4;
    const u32 t0 = ring + (c.lane >> 2) * 64 + ((gb & 1) * 32) + (c.lane & 3) * 8, t1 = t0 ^ 32;
    sfor<RING>([&](auto B) { issue(B, ring + CI(B) * 1024); });
    sfor<NB / 4>([&](auto C) {
        constexpr int ch = CI(C), issued = (RING + 4 * ch < NB) ? RING + 4 * ch : NB;
        wait_vm<issued - 4 * ch - 4>();
        sfor<4>([&](auto U) {
            constexpr int b = 4 * ch + CI(U);
            v2u lo = trd(t0 + (b % RING) * 1024), hi = trd(t1 + (b % RING) * 1024);
            sink(std::integral_constant<int, b>{}, lo, hi);
        });
        if constexpr (4 * ch + RING < NB) {
            wait_lgkm();
            sfor<4>([&](auto U) { constexpr int b = 4 * ch + RING + CI(U); issue(std::integral_constant<int, b>{}, ring + (b % RING) * 1024); });
        }
    });
    wait_lgkm();
}
DI u32 dma_chunk(u32 lane) { return ((lane & 3) ^ (((lane >> 4) & 1) << 1)) * 16; }
DI u32 row_f1(u32 lane) { const u32 gb = lane >> 4, r = (lane >> 2) & 3; return 16 * (gb >> 1) + 4 * (gb & 1) + (r & 1) + 8 * (r >> 1); }
DI u32 row_f2(u32 lane) { return 8 * (lane >> 4) + ((lane >> 2) & 3); }
DI u32 row_f3(u32 lane) { return lane >> 2; }
DI v8u cat8(v2u a, v2u b, v2u c, v2u d) { return v8u{a[0], a[1], b[0], b[1], c[0], c[1], d[0], d[1]}; }
DI v4u cat4(v2u a, v2u b) { return v4u{a[0], a[1], b[0], b[1]}; }

template <class USE> DI void eat_mem(Ctx& c, u32 base, USE use) {
    v4u m[10];
    const u32 vo = c.lane * 16;
    fence_c();
    sfor<10>([&](auto K) { m[CI(K)] = ld16(c.hot, vo + (kb(CI(K)) & 4095), base + (kb(CI(K)) & ~4095u)); });
    sfor<10>([&](auto K) {
        constexpr int ks = CI(K);
        u32 mx = umax3(umax3(m[ks][0], m[ks][1], m[ks][2]), m[ks][3], 0);
        if (__builtin_expect(__builtin_amdgcn_ballot_w64(mx == SENT) != 0, 0)) {
            u32 n = 0;
            for (;;) {
                fence_c();
                u32 mm = 0;
                sfor<10 - ks>([&](auto J) { constexpr int j = ks + CI(J); m[j] = ld16(c.hot, vo + (kb(j) & 4095), base + (kb(j) & ~4095u)); });
                sfor<10 - ks>([&](auto J) { constexpr int j = ks + CI(J); mm = umax3(mm, m[j][0], m[j][1]); mm = umax3(mm, m[j][2], m[j][3]); });
                if (__builtin_amdgcn_ballot_w64(mm == SENT) == 0) break;
                if (spin(c, n)) break;
            }
        }
        use(K, m[ks]);
    });
}
DI void wait_flags4(Ctx& c, u32 word0, u32 need) {
    u32 n = 0;
    for (;;) {
        fence_c();
        v4u f = ld16(c.flg, word0 * 4 + (c.lane & 31) * 16, 0);
        u32 mn = min(min(f[0], f[1]), min(f[2], f[3]));
        if (__builtin_amdgcn_ballot_w64(mn < need) == 0) break;
        if (spin(c, n)) break;
        __builtin_amdgcn_s_sleep(2);
    }
}

struct Args {
    const u16 *x, *mem0, *wq, *wk, *wv, *wo;
    u16* out;
    char* ws;
};

constexpr float SCALE = 0.08838834764831845f, LOG2E = 1.4426950408889634f;
DI float score(float s) { return bfr(bfr(s) * SCALE); }

DI void role_A(Ctx& c, const Args& a, u32 h) {
    const u32 lane = c.lane, w = c.wave;
    v8u W[10][4];
    {
        v2u t[10][4][4];
        const rsrc_t rq = mkrs(a.wq, 640u * 8192), rk = mkrs(a.wk, 640u * 8192);
        const u32 vo = row_f1(lane) * 8192 + (h * 128 + w * 32) * 2 + dma_chunk(lane);
        load_weights(c,
            [&](auto B, u32 dst) { constexpr int b = CI(B), m = b / 40, ks = (b % 40) / 4, tt = b % 4; dma16(dst, vo, m ? rk : rq, (64 * ks + 32 * (tt >> 1) + 2 * (tt & 1)) * 8192u); },
            [&](auto B, v2u lo, v2u hi) {
                constexpr int b = CI(B), m = b / 40, ks = (b % 40) / 4, tt = b % 4;
                t[ks][2 * m][tt] = lo; t[ks][2 * m + 1][tt] = hi;
                if constexpr (tt == 3) {
                    W[ks][2 * m] = cat8(t[ks][2 * m][0], t[ks][2 * m][1], t[ks][2 * m][2], t[ks][2 * m][3]);
                    W[ks][2 * m + 1] = cat8(t[ks][2 * m + 1][0], t[ks][2 * m + 1][1], t[ks][2 * m + 1][2], t[ks][2 * m + 1][3]);
                    if constexpr (ks < 8) { asm volatile("" : "+a"(W[ks][2 * m])); asm volatile("" : "+a"(W[ks][2 * m + 1])); }
                    else { asm volatile("" : "+v"(W[ks][2 * m])); asm volatile("" : "+v"(W[ks][2 * m + 1])); }
                }
            });
    }
    bar();
    constexpr u32 L_Q = 49152, L_K = 53248;
    auto prefetch = [&](u32 s) {
        u32 n = 0;
        for (;;) {
            fence_c();
            v4u f = ld16(c.flg, (F_QK + ((s & 1) * 32 + h) * 4) * 4, 0);
            u32 mn = rfl(min(min(f[0], f[1]), min(f[2], f[3])));
            if (mn > (s >> 1)) break;
            if (spin(c, n)) break;
            __builtin_amdgcn_s_sleep(2);
        }
        const u32 dst = (s % 3) * 16384, src = (s * 32 + h) * 8192;
        sfor<8>([&](auto I) { dma16c(dst + CI(I) * 1024, lane * 16, c.big, O_KX + src + CI(I) * 1024); });
        sfor<8>([&](auto I) { dma16c(dst + 8192 + CI(I) * 1024, lane * 16, c.big, O_QX + src + CI(I) * 1024); });
    };
    if (w == 3) { prefetch(0); prefetch(1); wait_vm<0>(); }
    bar();
    const u32 idx = (lane & 8) ? 0xEEEEu : 0x4444u;
    const u32 g = lane >> 4, part = (lane >> 3) & 1, r4 = (lane >> 2) & 3, c4 = lane & 3;
    const u32 ew = w * 32 + (lane >> 5) * 16 + (lane & 15), gp = (lane >> 4) & 1;
    u32 tpub = mt32();
    u32 sg = 0;
    for (u32 s = 0; s < NSEG; ++s) {
        const u32 kq = (s % 3) * 16384 + lane * 16;
        v4u kx[2][4];
        if (w < 3) sfor<8>([&](auto I) { kx[CI(I) >> 2][CI(I) & 3] = lr16(kq + CI(I) * 1024); });
        f32x4 acc[4] = {};
        if (DLY_A) delay(tpub, DLY_A);
        if (w == 0 && h == 0) { STAMP(0, s, 0); STAMPV(0, s, 4, mt32()); }
        eat_mem(c, mem_slot(c, sg), [&](auto K, v4u f) { sfor<4>([&](auto T) { acc[CI(T)] = smf(f, W[CI(K)][CI(T)], acc[CI(T)], idx); }); });
        if (w == 0 && h == 0) STAMP(0, s, 1);
        const u32 lq = L_Q + (s & 1) * 2048, lk = L_K + (s & 1) * 4096;
        {
            float q[4], k[4];
            sfor<4>([&](auto R) { q[CI(R)] = halfadd(acc[0][CI(R)], acc[1][CI(R)]); k[CI(R)] = halfadd(acc[2][CI(R)], acc[3][CI(R)]); });
            lw8(lq + ew * 16 + gp * 8, v2u{pk(q[0], q[1]), pk(q[2], q[3])});
            lw4(lk + ew * 32 + gp * 16, pk(k[0], k[1]));
            lw4(lk + ew * 32 + gp * 16 + 8, pk(k[2], k[3]));
        }
        wait_vm<0>();
        bar();
        if (w < 3) {
            v4u km[4], qf[4];
            sfor<4>([&](auto E) {
                constexpr int es = CI(E);
                const u32 ka = lk + (32 * es + 4 * g + r4) * 32 + c4 * 8;
                km[es] = cat4(trd(ka), trd(ka + 16 * 32));
            });
            if (w == 0) {
                sfor<4>([&](auto E) {
                    constexpr int es = CI(E);
                    const u32 qa = lq + (32 * es + 4 * g + r4) * 16 + (c4 & 1) * 8;
                    qf[es] = cat4(trd(qa), trd(qa + 16 * 16));
                });
            } else {
                sfor<4>([&](auto E) { qf[CI(E)] = lr16(kq + 8192 + (w - 1) * 4096 + CI(E) * 1024); });
            }
            f32x4 t0 = {}, t1 = {}, t2 = {};
            sfor<4>([&](auto E) { t1 = mfma(kx[0][CI(E)], qf[CI(E)], t1); t2 = mfma(kx[1][CI(E)], qf[CI(E)], t2); });
            sfor<4>([&](auto E) { t0 = mfma(km[CI(E)], qf[CI(E)], t0); });
            if (w == 0) {
                if (s + 1 < NSEG) {
                    float v[5];
                    sfor<4>([&](auto R) { v[CI(R)] = score(part ? t2[CI(R)] : t1[CI(R)]); });
                    v[4] = score(part ? t0[1] : t0[0]);
                    float mx = fmaxf(fmaxf(fmaxf(v[0], v[1]), fmaxf(v[2], v[3])), v[4]);
                    mx = fmaxf(mx, ror8(mx)); mx = max16(mx); mx = max32(mx);
                    const float mb = mx * LOG2E;
                    sfor<5>([&](auto R) { v[CI(R)] = __builtin_amdgcn_exp2f(v[CI(R)] * LOG2E - mb); });
                    float sm = (v[0] + v[1]) + (v[2] + v[3]) + v[4];
                    sm += ror8(sm); sm = sum16(sm); sm = sum32(sm);
                    const float inv = __builtin_amdgcn_rcpf(sm);
                    sfor<5>([&](auto R) { v[CI(R)] *= inv; });
                    const float o = ror8(v[4]);
                    const u32 pb = p_slot(c, sg), tq = lane & 7;
                    st8(v2u{canon(pk(v[0], v[1])), canon(pk(v[2], v[3]))}, c.hot, g * 128 + tq * 16 + part * 8, pb + (h >> 4) * 16384 + (h & 15) * 512);
                    if (part == 0) st4(canon(pk(v[4], o)), c.hot, tq * 16 + g * 4, pb + 32768 + h * 128);
                    tpub = mt32();
                    if (h == 0) STAMP(0, s, 2);
                    if (s > 0) {
                        const u32 po = p_slot(c, sg == 0 ? 2 : sg - 1);
                        const v4u e = {SENT, SENT, SENT, SENT};
                        if (lane < 32) st16(e, c.hot, lane * 16, po + (h >> 4) * 16384 + (h & 15) * 512);
                        else if (lane < 40) st16(e, c.hot, (lane - 32) * 16, po + 32768 + h * 128);
                    }
                }
            } else {
                const u32 qt = w - 1, q = qt * 16 + (lane & 15);
                float v[10];
                sfor<4>([&](auto R) {
                    v[CI(R)] = (4 * g + CI(R) <= q) ? score(t1[CI(R)]) : -1e9f;
                    v[4 + CI(R)] = (16 + 4 * g + CI(R) <= q) ? score(t2[CI(R)]) : -1e9f;
                });
                v[8] = score(t0[0]); v[9] = score(t0[1]);
                float mx = v[9];
                sfor<9>([&](auto R) { mx = fmaxf(mx, v[CI(R)]); });
                mx = max16(mx); mx = max32(mx);
                const float mb = mx * LOG2E;
                float sm = 0.f;
                sfor<10>([&](auto R) { v[CI(R)] = __builtin_amdgcn_exp2f(v[CI(R)] * LOG2E - mb); sm += v[CI(R)]; });
                sm = sum16(sm); sm = sum32(sm);
                const float inv = __builtin_amdgcn_rcpf(sm);
                sfor<10>([&](auto R) { v[CI(R)] *= inv; });
                const u32 e = (s * 2 + qt) * 32 + h;
                st16(v4u{pk(v[0], v[1]), pk(v[2], v[3]), pk(v[4], v[5]), pk(v[6], v[7])}, c.big, lane * 16, O_PU + e * 1024);
                st4(pk(v[8], v[9]), c.big, (lane & 15) * 16 + g * 4, O_PM + e * 256);
                tpub = mt32();
            }
        } else {
            if (lane == 0) st4(s, c.flg, (F_A + h) * 4, 0);
            if (s + 2 < NSEG) prefetch(s + 2);
            tpub = mt32();
        }
        sg = sg == 2 ? 0 : sg + 1;
    }
    wait_vm<0>();
    bar();
    if (w == 3 && lane == 0) st4(NSEG, c.flg, (F_A + h) * 4, 0);
}

DI void role_C(Ctx& c, const Args& a, u32 h) {
    const u32 lane = c.lane, w = c.wave;
    v8u Wv[10][2], Wo[2][10];
    {
        v2u t[2][4];
        const rsrc_t rv = mkrs(a.wv, 640u * 8192), ro = mkrs(a.wo, 4096u * 1280);
        const u32 vo = row_f1(lane) * 8192 + (h * 128 + w * 32) * 2 + dma_chunk(lane);
        const u32 vo2 = (h * 128 + row_f1(lane)) * 1280 + w * 320 + dma_chunk(lane);
        load_weights(c,
            [&](auto B, u32 dst) {
                constexpr int b = CI(B), tt = b % 4;
                if constexpr (b < 40) dma16(dst, vo, rv, (64 * (b / 4) + 32 * (tt >> 1) + 2 * (tt & 1)) * 8192u);
                else { constexpr int u = (b - 40) / 4, ks = u / 5, cp = u % 5; dma16(dst, vo2, ro, (64 * ks + 32 * (tt >> 1) + 2 * (tt & 1)) * 1280u + cp * 64); }
            },
            [&](auto B, v2u lo, v2u hi) {
                constexpr int b = CI(B), tt = b % 4;
                t[0][tt] = lo; t[1][tt] = hi;
                if constexpr (tt == 3) {
                    v8u w0 = cat8(t[0][0], t[0][1], t[0][2], t[0][3]), w1 = cat8(t[1][0], t[1][1], t[1][2], t[1][3]);
                    if constexpr (b < 64) { asm volatile("" : "+a"(w0)); asm volatile("" : "+a"(w1)); }
                    else { asm volatile("" : "+v"(w0)); asm volatile("" : "+v"(w1)); }
                    if constexpr (b < 40) { Wv[b / 4][0] = w0; Wv[b / 4][1] = w1; }
                    else { constexpr int u = (b - 40) / 4, ks = u / 5, cp = u % 5; Wo[ks][2 * cp] = w0; Wo[ks][2 * cp + 1] = w1; }
                }
            });
    }
    bar();
    const u32 idx = (lane & 8) ? 0xEEEEu : 0x4444u;
    const u32 g = lane >> 4, r4 = (lane >> 2) & 3, c4 = lane & 3;
    const u32 ew = w * 32 + (lane >> 5) * 16 + (lane & 15), gp = (lane >> 4) & 1;
    u32 yo[3], y2[3];
    sfor<3>([&](auto I) {
        constexpr int i = CI(I);
        const u32 tile = i < 2 ? 4 * i + 2 * (g & 1) + (g >> 1) : 8 + (g >> 1);
        const u32 col = 160 * w + 16 * tile + (lane & 15), j = col >> 3;
        yo[i] = (j >> 1) * 16384 + (j & 1) * 4096 + h * 128 + (col & 7) * 16;
        y2[i] = j * 4096 + h * 128 + (col & 7) * 16;
    });
    u32 tpub = mt32();
    u32 sg = 0;
    for (u32 s = 0; s < NSEG; ++s) {
        f32x4 acc[2] = {};
        if (DLY_A) delay(tpub, DLY_A);
        if (w == 0 && h == 0) STAMP(1, s, 0);
        eat_mem(c, mem_slot(c, sg), [&](auto K, v4u f) { sfor<2>([&](auto T) { acc[CI(T)] = smf(f, Wv[CI(K)][CI(T)], acc[CI(T)], idx); }); });
        if (w == 0 && h == 0) STAMP(1, s, 1);
        const u32 lv = (s & 1) * 2048;
        {
            float v[4];
            sfor<4>([&](auto R) { v[CI(R)] = halfadd(acc[0][CI(R)], acc[1][CI(R)]); });
            lw8(lv + ew * 16 + gp * 8, v2u{pk(v[0], v[1]), pk(v[2], v[3])});
        }
        wait_vm<0>();
        bar();
        if (w == 3 && lane == 0) st4(s, c.flg, (F_C + h) * 4, 0);
        v4u vf[2];
        sfor<2>([&](auto K) {
            const u32 va = lv + (64 * CI(K) + 16 * g + 8 * (c4 >> 1) + r4) * 16 + (c4 & 1) * 8;
            vf[CI(K)] = cat4(trd(va), trd(va + 64));
        });
        f32x4 d[10];
        sfor<10>([&](auto T) { f32x4 z = {}; z = smf(vf[0], Wo[0][CI(T)], z, idx); d[CI(T)] = smf(vf[1], Wo[1][CI(T)], z, idx); });
        u32 pa[5][2];
        sfor<5>([&](auto P) {
            constexpr int p = CI(P);
            float y[4];
            sfor<4>([&](auto R) { y[CI(R)] = halfadd(d[2 * p][CI(R)], d[2 * p + 1][CI(R)]); });
            pa[p][0] = canon(pk(y[0], y[1])); pa[p][1] = canon(pk(y[2], y[3]));
        });
        v4u ch[3];
        sfor<3>([&](auto I) {
            constexpr int i = CI(I), pA = 2 * i, pB = i < 2 ? 2 * i + 1 : 4;
            auto r0 = __builtin_amdgcn_permlane16_swap(pa[pA][0], pa[pB][0], false, false);
            auto r1 = __builtin_amdgcn_permlane16_swap(pa[pA][1], pa[pB][1], false, false);
            ch[i] = v4u{r0[0], r1[0], r0[1], r1[1]};
        });
        const bool last = (g & 1) == 0;
        if (s + 1 < NSEG) {
            const u32 yb = y_slot(c, sg);
            st16(ch[0], c.hot, yo[0], yb); st16(ch[1], c.hot, yo[1], yb);
            if (last) st16(ch[2], c.hot, yo[2], yb);
        }
        tpub = mt32();
        if (w == 0 && h == 0) STAMP(1, s, 2);
        st16(ch[0], c.big, y2[0], O_Y2 + s * 327680); st16(ch[1], c.big, y2[1], O_Y2 + s * 327680);
        if (last) st16(ch[2], c.big, y2[2], O_Y2 + s * 327680);
        if (s > 0 && s + 1 < NSEG) {
            const u32 yb = y_slot(c, sg == 0 ? 2 : sg - 1);
            const v4u e = {SENT, SENT, SENT, SENT};
            st16(e, c.hot, yo[0], yb); st16(e, c.hot, yo[1], yb);
            if (last) st16(e, c.hot, yo[2], yb);
        }
        sg = sg == 2 ? 0 : sg + 1;
    }
    wait_vm<0>();
    bar();
    if (w == 3 && lane == 0) st4(NSEG, c.flg, (F_C + h) * 4, 0);
}

DI void role_B(Ctx& c, const Args& a, u32 b) {
    const u32 lane = c.lane, w = c.wave, j = 2 * b + (w & 1);
    const u32 mline = (j >> 6) * 16384 + (j & 63) * 128;
    if (w >= 2) {
        auto fetch = [&](u32 s) {
            wait_flags4(c, F_VU + (s & 1) * 128, (s >> 1) + 1);
            const u32 dst = (s & 3) * 32768 + (w & 1) * 16384, src = O_U + (s * 80 + j) * 16384;
            sfor<16>([&](auto I) { dma16c(dst + CI(I) * 1024, lane * 16, c.big, src + CI(I) * 1024); });
        };
        fetch(0); fetch(1); fetch(2);
        wait_vm<32>();
        bar();
        for (u32 s = 0; s + 1 < NSEG; ++s) {
            if (s + 3 < NSEG - 1) { fetch(s + 3); wait_vm<16>(); } else wait_vm<0>();
            bar();
        }
    } else {
        if (lane < 8) {
            const rsrc_t rm = mkrs(a.mem0, 8 * 1280);
            v4u v = __builtin_bit_cast(v4u, __builtin_amdgcn_raw_buffer_load_b128(rm, lane * 1280 + j * 16, 0, 0));
            st16(v4u{canon(v[0]), canon(v[1]), canon(v[2]), canon(v[3])}, c.hot, lane * 16, mem_slot(c, 0) + mline);
        }
        bar();
        u32 sg = 0, tpub = mt32();
        const u32 vo = lane * 16;
        const u32 so = (lane & 7) * 16 + ((lane >> 4) & 1) * 8 + (lane >> 5) * 4;
        for (u32 s = 0; s + 1 < NSEG; ++s) {
            v4u z[16];
            sfor<16>([&](auto I) { z[CI(I)] = lr16((s & 3) * 32768 + w * 16384 + CI(I) * 1024 + vo); });
            const u32 yb = y_slot(c, sg) + (j >> 1) * 16384 + (j & 1) * 4096, pb = p_slot(c, sg);
            wait_lgkm();
            if (DLY_B) delay(tpub, DLY_B);
            if (j == 0) STAMP(2, s, 0);
            v4u y[4], p[20];
            f32x4 acc = {};
            u32 n = 0;
            for (;;) {
                fence_c();
                sfor<4>([&](auto I) { y[CI(I)] = ld16(c.hot, vo + CI(I) * 1024, yb); });
                sfor<20>([&](auto I) { p[CI(I)] = ld16(c.hot, vo + (kb(CI(I)) & 4095), pb + (kb(CI(I)) & ~4095u)); });
                u32 mm = 0;
                sfor<4>([&](auto I) { mm = umax3(mm, y[CI(I)][0], y[CI(I)][1]); mm = umax3(mm, y[CI(I)][2], y[CI(I)][3]); });
                acc = f32x4{};
                sfor<20>([&](auto I) {
                    constexpr int i = CI(I);
                    mm = umax3(mm, p[i][0], p[i][1]); mm = umax3(mm, p[i][2], p[i][3]);
                    if constexpr (i < 16) acc = mfma(z[i], p[i], acc); else acc = mfma(y[i - 16], p[i], acc);
                });
                if (__builtin_amdgcn_ballot_w64(mm == SENT) == 0) break;
                if (spin(c, n)) break;
            }
            if (j == 0) STAMPV(2, s, 3, n);
            auto r02 = __builtin_amdgcn_permlane32_swap(u_(acc[0]), u_(acc[2]), false, false);
            auto r13 = __builtin_amdgcn_permlane32_swap(u_(acc[1]), u_(acc[3]), false, false);
            const float s02 = f_(r02[0]) + ror8(f_(r02[1])), s13 = f_(r13[0]) + ror8(f_(r13[1]));
            const u32 nx = sg == 2 ? 0 : sg + 1;
            if ((lane & 8) == 0) st4(canon(pk(s02, s13)), c.hot, so, mem_slot(c, nx) + mline);
            tpub = mt32();
            if (j == 0) STAMP(2, s, 1);
            if (lane < 8) st16(v4u{SENT, SENT, SENT, SENT}, c.hot, lane * 16, mem_slot(c, sg) + mline);
            sg = nx;
            bar();
        }
    }
}

constexpr u32 L_V = 122880;
DI void stage_x(const Ctx& c, rsrc_t rx, u32 s, u32 slot) {
    const u32 tt = c.wave >> 1, ks0 = (c.wave & 1) * 10;
    const u32 vo = (tt * 16 + (c.lane & 15)) * 1280 + (c.lane >> 4) * 16 + ks0 * 64;
    sfor<10>([&](auto I) { dma16(slot * 40960 + c.wave * 10240 + CI(I) * 1024, vo, rx, s * 40960 + CI(I) * 64); });
}
DI void role_QK(Ctx& c, const Args& a, u32 h, u32 pi) {
    const u32 lane = c.lane, w = c.wave;
    v4u W[20][4];
    {
        v2u t[2][2];
        const rsrc_t rq = mkrs(a.wq, 640u * 8192), rk = mkrs(a.wk, 640u * 8192);
        const u32 vo = row_f2(lane) * 8192 + (h * 128 + w * 32) * 2 + dma_chunk(lane);
        load_weights(c,
            [&](auto B, u32 dst) { constexpr int b = CI(B), m = (b >> 1) & 1, ks = b >> 2, tt = b & 1; dma16(dst, vo, m ? rq : rk, (32 * ks + 4 * tt) * 8192u); },
            [&](auto B, v2u lo, v2u hi) {
                constexpr int b = CI(B), m = (b >> 1) & 1, ks = b >> 2, tt = b & 1;
                t[0][tt] = lo; t[1][tt] = hi;
                if constexpr (tt == 1) {
                    v4u w0 = cat4(t[0][0], t[0][1]), w1 = cat4(t[1][0], t[1][1]);
                    if constexpr (ks < 16) { asm volatile("" : "+a"(w0)); asm volatile("" : "+a"(w1)); }
                    else { asm volatile("" : "+v"(w0)); asm volatile("" : "+v"(w1)); }
                    W[ks][2 * m] = w0; W[ks][2 * m + 1] = w1;
                }
            });
    }
    bar();
    const rsrc_t rx = mkrs(a.x, 2048u * 1280);
    stage_x(c, rx, pi, 0); stage_x(c, rx, pi + 2, 1);
    u32 slot = 0;
    for (u32 n = 0; n < NSEG / 2; ++n) {
        const u32 s = 2 * n + pi;
        if (n == 0) wait_vm<10>(); else wait_vm<15>();
        bar();
        if (n + 2 < NSEG / 2) stage_x(c, rx, s + 4, slot == 0 ? 2 : slot - 1); else stage_x(c, rx, s, slot == 0 ? 2 : slot - 1);
        f32x4 acc[4][2] = {};
        const u32 xb = slot * 40960 + lane * 16;
        sfor<20>([&](auto K) {
            constexpr int ks = CI(K);
            const v4u x0 = lr16(xb + ks * 1024), x1 = lr16(xb + (20 + ks) * 1024);
            sfor<4>([&](auto T) { acc[CI(T)][0] = mfma(W[ks][CI(T)], x0, acc[CI(T)][0]); acc[CI(T)][1] = mfma(W[ks][CI(T)], x1, acc[CI(T)][1]); });
            if constexpr (ks == 7) { if (n > 0) { wait_vm<10>(); } if (lane == 0) st4(n, c.flg, (F_QK + (pi * 32 + h) * 4 + w) * 4, 0); }
        });
        sfor<4>([&](auto I) {
            constexpr int m = CI(I) >> 1, tt = CI(I) & 1;
            const f32x4 lo = acc[2 * m][tt], hi = acc[2 * m + 1][tt];
            st16(v4u{pk(lo[0], lo[1]), pk(lo[2], lo[3]), pk(hi[0], hi[1]), pk(hi[2], hi[3])}, c.big, lane * 16 + w * 1024, (m ? O_QX : O_KX) + (s * 32 + h) * 8192 + tt * 4096);
        });
        if (n == 0) { wait_vm<0>(); if (lane == 0) st4(1, c.flg, (F_QK + (pi * 32 + h) * 4 + w) * 4, 0); }
        slot = slot == 2 ? 0 : slot + 1;
    }
    wait_vm<0>();
    if (lane == 0) st4(NSEG / 2, c.flg, (F_QK + (pi * 32 + h) * 4 + w) * 4, 0);
}
DI void role_VU(Ctx& c, const Args& a, u32 h, u32 pi) {
    const u32 lane = c.lane, w = c.wave;
    v4u Wv[20][2], Wo[4][10];
    {
        v2u t[2][2];
        const rsrc_t rv = mkrs(a.wv, 640u * 8192), ro = mkrs(a.wo, 4096u * 1280);
        const u32 vo = row_f2(lane) * 8192 + (h * 128 + w * 32) * 2 + dma_chunk(lane);
        const u32 vo2 = (h * 128 + row_f3(lane)) * 1280 + w * 320 + dma_chunk(lane);
        load_weights(c,
            [&](auto B, u32 dst) {
                constexpr int b = CI(B), tt = b & 1;
                if constexpr (b < 40) dma16(dst, vo, rv, (32 * (b >> 1) + 4 * tt) * 8192u);
                else { constexpr int u = (b - 40) >> 1, es = u / 5, cp = u % 5; dma16(dst, vo2, ro, (32 * es + 16 * tt) * 1280u + cp * 64); }
            },
            [&](auto B, v2u lo, v2u hi) {
                constexpr int b = CI(B), tt = b & 1;
                t[0][tt] = lo; t[1][tt] = hi;
                if constexpr (tt == 1) {
                    v4u w0 = cat4(t[0][0], t[0][1]), w1 = cat4(t[1][0], t[1][1]);
                    if constexpr (b < 64) { asm volatile("" : "+a"(w0)); asm volatile("" : "+a"(w1)); }
                    else { asm volatile("" : "+v"(w0)); asm volatile("" : "+v"(w1)); }
                    if constexpr (b < 40) { Wv[b >> 1][0] = w0; Wv[b >> 1][1] = w1; }
                    else { constexpr int u = (b - 40) >> 1, es = u / 5, cp = u % 5; Wo[es][2 * cp] = w0; Wo[es][2 * cp + 1] = w1; }
                }
            });
    }
    bar();
    const rsrc_t rx = mkrs(a.x, 2048u * 1280);
    stage_x(c, rx, pi, 0); stage_x(c, rx, pi + 2, 1);
    const u32 uo = ((20 * w + ((lane >> 3) & 1)) * 32 + h) * 512 + (lane >> 4) * 128 + (lane & 7) * 16;
    u32 slot = 0;
    for (u32 n = 0; n < NSEG / 2; ++n) {
        const u32 s = 2 * n + pi;
        if (n == 0) wait_vm<10>(); else wait_vm<21>();
        bar();
        if (n + 2 < NSEG / 2) stage_x(c, rx, s + 4, slot == 0 ? 2 : slot - 1); else stage_x(c, rx, s, slot == 0 ? 2 : slot - 1);
        f32x4 acc[2][2] = {};
        const u32 xb = slot * 40960 + lane * 16;
        sfor<20>([&](auto K) {
            constexpr int ks = CI(K);
            const v4u x0 = lr16(xb + ks * 1024), x1 = lr16(xb + (20 + ks) * 1024);
            sfor<2>([&](auto T) { acc[CI(T)][0] = mfma(Wv[ks][CI(T)], x0, acc[CI(T)][0]); acc[CI(T)][1] = mfma(Wv[ks][CI(T)], x1, acc[CI(T)][1]); });
        });
        const u32 lv = L_V + (n & 1) * 8192;
        sfor<2>([&](auto T) {
            constexpr int tt = CI(T);
            lw16(lv + tt * 4096 + w * 1024 + lane * 16, v4u{pk(acc[0][tt][0], acc[0][tt][1]), pk(acc[0][tt][2], acc[0][tt][3]), pk(acc[1][tt][0], acc[1][tt][1]), pk(acc[1][tt][2], acc[1][tt][3])});
        });
        if (n > 0) wait_vm<10>();
        if (lane == 0) st4(n, c.flg, (F_VU + (pi * 32 + h) * 4 + w) * 4, 0);
        bar();
        v4u vf[2][4];
        sfor<8>([&](auto I) { vf[CI(I) >> 2][CI(I) & 3] = lr16(lv + CI(I) * 1024 + lane * 16); });
        sfor<10>([&](auto T) {
            constexpr int ct = CI(T);
            f32x4 u0 = {}, u1 = {};
            sfor<4>([&](auto E) { u0 = mfma(vf[0][CI(E)], Wo[CI(E)][ct], u0); u1 = mfma(vf[1][CI(E)], Wo[CI(E)][ct], u1); });
            st16(v4u{pk(u0[0], u0[1]), pk(u0[2], u0[3]), pk(u1[0], u1[1]), pk(u1[2], u1[3])}, c.big, uo, O_U + (s * 80 + 2 * ct) * 16384);
        });
        if (n == 0) { wait_vm<0>(); if (lane == 0) st4(1, c.flg, (F_VU + (pi * 32 + h) * 4 + w) * 4, 0); }
        slot = slot == 2 ? 0 : slot + 1;
    }
    wait_vm<0>();
    if (lane == 0) st4(NSEG / 2, c.flg, (F_VU + (pi * 32 + h) * 4 + w) * 4, 0);
}
DI void role_F(Ctx& c, const Args& a, u32 id) {
    const u32 lane = c.lane, w = c.wave, g = lane >> 4, i16 = lane & 15;
    const rsrc_t ro = mkrs(a.out, 2048u * 1280);
    bar();
    if (w == 0) { wait_flags4(c, F_VU, NSEG / 2); wait_flags4(c, F_VU + 128, NSEG / 2); }
    u32 par = 0;
    for (u32 task = id; task < NSEG * 20; task += 128) {
        const u32 s = task / 20, cg = task % 20;
        if (w == 0) {
            u32 n = 0;
            for (;;) {
                fence_c();
                const u32 f = ld4(c.flg, (lane < 32 ? F_A + lane : F_C + lane - 32) * 4, 0);
                if (__builtin_amdgcn_ballot_w64(f <= s) == 0) break;
                if (spin(c, n)) break;
                __builtin_amdgcn_s_sleep(4);
            }
        }
        bar();
        const u32 jj = s * 80 + 4 * cg + (i16 >> 2);
        const u32 uo = (jj * 32 + 8 * w) * 512 + g * 128 + (i16 & 3) * 16;
        const u32 yo = (jj * 32 + 8 * w + g) * 128 + (i16 & 3) * 16;
        const u32 po = (s * 64 + 8 * w) * 1024 + lane * 16;
        const u32 mo = (s * 64 + 8 * w + g) * 256 + i16 * 16;
        v4u za[10][2], pb[10][2];
        sfor<8>([&](auto K) {
            constexpr int k = CI(K);
            za[k][0] = ld16(c.big, uo + k * 512, O_U); za[k][1] = ld16(c.big, uo + k * 512 + 64, O_U);
            pb[k][0] = ld16(c.big, po, O_PU + k * 1024); pb[k][1] = ld16(c.big, po, O_PU + 32768 + k * 1024);
        });
        sfor<2>([&](auto K) {
            constexpr int k = CI(K);
            za[8 + k][0] = ld16(c.big, yo + k * 512, O_Y2); za[8 + k][1] = ld16(c.big, yo + k * 512 + 64, O_Y2);
            pb[8 + k][0] = ld16(c.big, mo + k * 1024, O_PM); pb[8 + k][1] = ld16(c.big, mo + k * 1024, O_PM + 8192);
        });
        f32x4 acc[2][2] = {};
        sfor<10>([&](auto K) { sfor<4>([&](auto I) { constexpr int ct = CI(I) & 1, qt = CI(I) >> 1; acc[qt][ct] = mfma(za[CI(K)][ct], pb[CI(K)][qt], acc[qt][ct]); }); });
        const u32 lb = par * 16384;
        sfor<4>([&](auto I) { lw16(lb + (w * 4 + CI(I)) * 1024 + lane * 16, __builtin_bit_cast(v4u, acc[CI(I) >> 1][CI(I) & 1])); });
        bar();
        if (w < 2) {
            f32x4 r0 = {}, r1 = {};
            sfor<4>([&](auto V) {
                r0 += __builtin_bit_cast(f32x4, lr16(lb + (CI(V) * 4 + w * 2) * 1024 + lane * 16));
                r1 += __builtin_bit_cast(f32x4, lr16(lb + (CI(V) * 4 + w * 2 + 1) * 1024 + lane * 16));
            });
            const v4u o = {pk(r0[0], r0[1]), pk(r0[2], r0[3]), pk(r1[0], r1[1]), pk(r1[2], r1[3])};
            __builtin_amdgcn_raw_buffer_store_b128(o, ro, (s * 32 + w * 16 + i16) * 1280 + cg * 64 + g * 16, 0, 0);
        }
        par ^= 1;
    }
}

DI char* hot_base(char* ws) { return (char*)(((u64)ws + FLAG_BYTES + HOT_BYTES - 1) & ~(u64)(HOT_BYTES - 1)); }
DI u32 xcc_id() { return (u32)__builtin_amdgcn_s_getreg(20 | (3 << 11)) & 0xfu; }

__global__ void __launch_bounds__(256) k_init(char* ws) {
    const u32 tid = blockIdx.x * 256 + threadIdx.x;
    char* hot = hot_base(ws);
    const rsrc_t rh = mkrs(hot, HOT_BYTES), rf = mkrs(ws, FLAG_BYTES);
    for (u32 o = tid * 16; o < 135u * 16384; o += gridDim.x * 4096) st16(v4u{SENT, SENT, SENT, SENT}, rh, o < 95u * 16384 ? o : o + 33u * 16384, 0);
    if (tid < 512 && !(tid * 4 >= F_PAR && tid * 4 < F_PAR + 4)) st16(v4u{0, 0, 0, 0}, rf, tid * 16, 0);
    if (blockIdx.x == 0 && threadIdx.x < 64) {
        const u32 x = xcc_id();
        u32 t[4] = {0, 0, 0, 0}, sink = 0;
        if (threadIdx.x < 32) st4(0u, rh, ((threadIdx.x >> 1) & 1) * (2u << 20) + (100u + (threadIdx.x >> 2)) * 16384u + (threadIdx.x & 1) * 8192u + (threadIdx.x >> 2) * 1152u, 0);
        wait_vm<0>();
        for (u32 rep = 0; rep < 8; ++rep)
            for (u32 q = 0; q < 4; ++q) {
                const u32 off = (q >> 1) * (2u << 20) + (100u + rep) * 16384u + (q & 1) * 8192u + rep * 1152u;
                const u32 t0 = mt32();
                asm volatile("s_waitcnt lgkmcnt(0)" ::: "memory");
                u32 v = ld4(rh, off, 0);
                asm volatile("s_waitcnt vmcnt(0)" : "+v"(v)::"memory");
                const u32 t1 = mt32();
                sink += v;
                if (rep > 0) t[q] += t1 - t0;
            }
        const u32 flip = x >= 4 ? 1u : 0u;
        if (threadIdx.x == 0) {
            st4(((t[1] < t[0]) ? 1u : 0u) ^ flip, rf, F_PAR * 4, 0);
            st4(((t[3] < t[2]) ? 1u : 0u) ^ flip, rf, (F_PAR + 1) * 4, 0);
            st4(t[0] | (t[1] << 16), rf, (F_PAR + 2) * 4, 0);
            st4((t[2] | (t[3] << 16)) + (sink == 0x12345u), rf, (F_PAR + 3) * 4, 0);
        }
    }
}

__global__ void __launch_bounds__(256, 1) k_main(Args a) {
    extern __shared__ __attribute__((aligned(16))) char lds_[];
    Ctx c;
    c.lane = threadIdx.x & 63; c.wave = rfl(threadIdx.x >> 6); c.dead = 0;
    char* hot = hot_base(a.ws);
    c.flg = mkrs(a.ws, FLAG_BYTES); c.hot = mkrs(hot, HOT_BYTES); c.big = mkrs(hot + HOT_BYTES, BIG_BYTES);
    u32* fl = (u32*)a.ws;
    if (threadIdx.x == 0) {
        const u32 x = xcc_id();
        const u32 quota = x < 4 ? 26u : (x < 8 ? 32u : 0u);
        const u32 r = __hip_atomic_fetch_add(&fl[F_TICK + x * 32], 1u, __ATOMIC_RELAXED, __HIP_MEMORY_SCOPE_AGENT);
        u32 role = x * 32 + r;
        if (r >= quota) {
            const u32 k = __hip_atomic_fetch_add(&fl[F_SPARE], 1u, __ATOMIC_RELAXED, __HIP_MEMORY_SCOPE_AGENT);
            u32 cnt[16], n = 0;
            for (;;) {
                u32 tot = 0;
                sfor<16>([&](auto I) { cnt[CI(I)] = __hip_atomic_load(&fl[F_TICK + CI(I) * 32], __ATOMIC_RELAXED, __HIP_MEMORY_SCOPE_AGENT); tot += cnt[CI(I)]; });
                if (tot >= gridDim.x || ++n > SPIN_CAP) break;
                __builtin_amdgcn_s_sleep(8);
            }
            role = 0xFFFFu;
            u32 left = k;
            sfor<8>([&](auto I) {
                constexpr u32 q = CI(I) < 4 ? 26u : 32u;
                const u32 miss = cnt[CI(I)] < q ? q - cnt[CI(I)] : 0u;
                if (role == 0xFFFFu) { if (left < miss) role = CI(I) * 32 + cnt[CI(I)] + left; else left -= miss; }
            });
        }
        lw4(LDS_BYTES - 16, role);
    }
    c.par0 = rfl(ld4(c.flg, F_PAR * 4, 0)) & 1; c.par1 = rfl(ld4(c.flg, (F_PAR + 1) * 4, 0)) & 1;
    bar();
    const u32 role = rfl(*(LAS u32*)(LDS_BYTES - 16));
    bar();
    if (role == 0xFFFFu) return;
    const u32 x = role >> 5, r = role & 31;
    if (x < 4) {
        if (r < 8) role_A(c, a, 8 * x + r);
        else if (r < 16) role_C(c, a, 8 * x + r - 8);
        else role_B(c, a, 10 * x + r - 16);
    } else {
        const u32 h = 8 * (x - 4) + (r & 7), kind = r >> 3;
        if (kind < 2) role_QK(c, a, h, kind); else role_VU(c, a, h, kind - 2);
        role_F(c, a, role - 128);
    }
    (void)lds_;
}

extern "C" void kernel_launch(void* const* d_in, const int* in_sizes, int n_in, void* d_out, int out_size, void* d_ws, size_t ws_size, hipStream_t stream) {
    (void)in_sizes; (void)n_in; (void)out_size; (void)ws_size;
    Args a;
    a.x = (const u16*)d_in[0]; a.mem0 = (const u16*)d_in[1]; a.wq = (const u16*)d_in[2]; a.wk = (const u16*)d_in[3];
    a.wv = (const u16*)d_in[4]; a.wo = (const u16*)d_in[5]; a.out = (u16*)d_out; a.ws = (char*)d_ws;
    (void)hipFuncSetAttribute((const void*)k_main, hipFuncAttributeMaxDynamicSharedMemorySize, LDS_BYTES);
    k_init<<<64, 256, 0, stream>>>((char*)d_ws);
    k_main<<<256, 256, LDS_BYTES, stream>>>(a);
}
```

```cpp
#include <hip/hip_runtime.h>
#include <hip/hip_bf16.h>
#include <utility>

typedef unsigned u32;
typedef unsigned long long u64;
typedef unsigned short u16;
typedef __attribute__((ext_vector_type(2))) __bf16 bf16x2;
typedef __attribute__((ext_vector_type(8))) __bf16 bf16x8;
typedef __attribute__((ext_vector_type(16))) __bf16 bf16x16;
typedef __attribute__((ext_vector_type(4))) float f32x4;
typedef __attribute__((ext_vector_type(4))) short s16x4;
typedef __attribute__((ext_vector_type(2))) u32 v2u;
typedef __attribute__((ext_vector_type(4))) u32 v4u;
typedef __attribute__((ext_vector_type(8))) u32 v8u;
typedef __amdgpu_buffer_rsrc_t rsrc_t;
#define DI __device__ __forceinline__
#define LAS __attribute__((address_space(3)))

constexpr int NSEG = 64;
constexpr u32 SENT = 0xFFFFFFFFu;
constexpr int SC1 = 16;
constexpr u32 OOB = 0x80000000u;
constexpr int F_CH = 0;
constexpr int F_RS = 256;
constexpr int F_ZERO = 512;
constexpr int F_DONE = 640;
constexpr int F_PAR = 544;
constexpr int F_ERR = 576;
constexpr int F_QK = 1024;
constexpr int F_VU = 1280;
constexpr int F_A = 2048;
constexpr int F_C = 3072;
constexpr int F_DBG = 4096;
constexpr u32 FLAG_BYTES = 1u << 20;
constexpr u32 H_QX = 8192, H_Y2 = 16384, H_PU = 26624, H_PM = 28672, O_U = 64u << 20;
constexpr u32 O_X = O_U + (80u << 20), BIG_BYTES = O_X + (26u << 20);
constexpr u32 HOT_BYTES = 4u << 20;
constexpr u32 LDS_BYTES = 160 * 1024;

template <class F, int... I> DI void sfor_(F&& f, std::integer_sequence<int, I...>) { (f(std::integral_constant<int, I>{}), ...); }
template <int N, class F> DI void sfor(F&& f) { sfor_(f, std::make_integer_sequence<int, N>{}); }
#define CI(I) decltype(I)::value

DI u32 rfl(u32 x) { u32 r; asm volatile("s_nop 1\n\tv_readfirstlane_b32 %0, %1\n\ts_nop 4" : "=s"(r) : "v"(x)); return r; }
DI u32 pk(float a, float b) { bf16x2 v = {(__bf16)a, (__bf16)b}; return __builtin_bit_cast(u32, v); }
DI u32 canon(u32 x) { return x < 0xFFFFFFFEu ? x : 0xFFFFFFFEu; }
DI float bfr(float x) { return (float)(__bf16)x; }
DI u32 umax3(u32 a, u32 b, u32 c) { u32 m = a > b ? a : b; return m > c ? m : c; }
DI rsrc_t mkrs(const void* p, u32 bytes) { return __builtin_amdgcn_make_buffer_rsrc((void*)p, 0, bytes, 0x00020000); }
DI v4u ld16(rsrc_t r, u32 vo, u32 so) { return __builtin_bit_cast(v4u, __builtin_amdgcn_raw_buffer_load_b128(r, vo, so, SC1)); }
DI u32 ld4(rsrc_t r, u32 vo, u32 so) { return __builtin_amdgcn_raw_buffer_load_b32(r, vo, so, SC1); }
DI void st16(v4u v, rsrc_t r, u32 vo, u32 so) { __builtin_amdgcn_raw_buffer_store_b128(v, r, vo, so, SC1); }
DI void st8(v2u v, rsrc_t r, u32 vo, u32 so) { __builtin_amdgcn_raw_buffer_store_b64(v, r, vo, so, SC1); }
DI void st4(u32 v, rsrc_t r, u32 vo, u32 so) { __builtin_amdgcn_raw_buffer_store_b32(v, r, vo, so, SC1); }
template <int N> DI void wait_vm() { asm volatile("s_waitcnt vmcnt(%0)" ::"i"(N) : "memory"); }
DI void wait_lgkm() { asm volatile("s_waitcnt lgkmcnt(0)" ::: "memory"); }
DI void bar() { asm volatile("s_waitcnt lgkmcnt(0)\n\ts_barrier" ::: "memory"); }
DI void fence_c() { asm volatile("" ::: "memory"); }
DI void dma16(u32 lds, u32 vo, rsrc_t r, u32 so) {
    asm volatile("s_mov_b32 m0, %0\n\ts_nop 0\n\tbuffer_load_dwordx4 %1, %2, %3 offen lds" ::"s"(lds), "v"(vo), "s"(r), "s"(so) : "memory", "m0");
}
DI void dma16c(u32 lds, u32 vo, rsrc_t r, u32 so) {
    asm volatile("s_mov_b32 m0, %0\n\ts_nop 0\n\tbuffer_load_dwordx4 %1, %2, %3 offen sc1 lds" ::"s"(lds), "v"(vo), "s"(r), "s"(so) : "memory", "m0");
}
DI v2u trd(u32 a) { return __builtin_bit_cast(v2u, __builtin_amdgcn_ds_read_tr16_b64_v4i16((LAS s16x4*)a)); }
DI v4u lr16(u32 a) { return *(LAS v4u*)a; }
DI void lw16(u32 a, v4u v) { *(LAS v4u*)a = v; }
DI void lw8(u32 a, v2u v) { *(LAS v2u*)a = v; }
DI void lw4(u32 a, u32 v) { *(LAS u32*)a = v; }
DI f32x4 mfma(v4u a, v4u b, f32x4 c) { return __builtin_amdgcn_mfma_f32_16x16x32_bf16(__builtin_bit_cast(bf16x8, a), __builtin_bit_cast(bf16x8, b), c, 0, 0, 0); }
DI f32x4 smf(v4u a, v8u b, f32x4 c, u32 idx) { return __builtin_amdgcn_smfmac_f32_16x16x64_bf16(__builtin_bit_cast(bf16x8, a), __builtin_bit_cast(bf16x16, b), c, (int)idx, 0, 0); }
DI float f_(u32 x) { return __uint_as_float(x); }
DI u32 u_(float x) { return __float_as_uint(x); }
DI float ror8(float x) { return f_((u32)__builtin_amdgcn_update_dpp(0, (int)u_(x), 0x128, 0xf, 0xf, false)); }
DI u32 ror8u(u32 x) { return (u32)__builtin_amdgcn_update_dpp(0, (int)x, 0x128, 0xf, 0xf, false); }
DI float halfadd(float x, float y) { auto r = __builtin_amdgcn_permlane32_swap(u_(x), u_(y), false, false); return f_(r[0]) + f_(r[1]); }
DI float max16(float x) { auto r = __builtin_amdgcn_permlane16_swap(u_(x), u_(x), false, false); return fmaxf(f_(r[0]), f_(r[1])); }
DI float max32(float x) { auto r = __builtin_amdgcn_permlane32_swap(u_(x), u_(x), false, false); return fmaxf(f_(r[0]), f_(r[1])); }
DI float sum16(float x) { auto r = __builtin_amdgcn_permlane16_swap(u_(x), u_(x), false, false); return f_(r[0]) + f_(r[1]); }
DI float sum32(float x) { auto r = __builtin_amdgcn_permlane32_swap(u_(x), u_(x), false, false); return f_(r[0]) + f_(r[1]); }
DI u64 rt() { return __builtin_amdgcn_s_memrealtime(); }
DI u32 mt32() { return (u32)__builtin_amdgcn_s_memtime(); }

constexpr u32 SPIN_CAP = 1u << 18;
struct Ctx {
    rsrc_t hot, big, flg;
    u32 lane, wave, par0, par1, l0;
    u32 dead, role, id, word;
};
#define RM 63
#define SKEW 0
DI u32 hf(const Ctx&, u32 h, u32 s) { return (h << 21) + (((s + SKEW * h) & RM) << 15); }
DI u32 hn(const Ctx& c, u32 h, u32 s) { return O_X - H_Y2 + (s & 63) * 409600u + h * 12800u; }
DI u32 y2(u32 u) { return H_Y2 + u * 256; }
DI u32 us(const Ctx& c, u32 u, u32 s) { return O_U + hf(c, u, s + (SKEW ? 34 : 0)); }
DI void flag(const Ctx& c, u32 v, u32 word) { st4(v, c.flg, c.l0, word * 4); }
DI bool spin(Ctx& c, u32& n) {
    if (c.dead) return true;
    if (++n > SPIN_CAP) { c.dead = 1; flag(c, 1u, F_ERR); return true; }
    return false;
}
#define TQ 84
#define TP1 178
#define TF1 109
#define TP2 166
#define TF2 103
#define TS0 35
DI u32 rt32() { return (u32)rt(); }
#define TQv TQ
#define TQc "i"
#define TPv(s) ((s) < TS0 ? TP1 : TP2)
#define TFv(s) ((s) < TS0 ? TF1 : TF2)
struct Clk { u32 F, pr, pm, rho, qr, qm; };
#define LATE 8
DI void clk_init(const Ctx& c, Clk& k, u32 phase) {
    const u32 n = rt32();
    k.F = rfl(n - n % TQv + phase - 4 * TQv); k.pr = k.qr = n; k.pm = k.qm = mt32(); k.rho = 6000;
}
DI u32 clk_mode(const Ctx& c, Clk& k, bool timed) {
    if (!timed) return rfl(0u);
    const u32 n = rt32() + LATE;
    while ((int)(k.F - n) < 0) k.F += TQv;
    return rfl((int)(k.F - n) < 20 - LATE ? 2u : 1u);
}
DI void clk_next(const Ctx& c, Clk& k, u32 d) {
    k.F += d;
    const u32 dr = k.pr - k.qr;
    if (dr >= 64u) { k.rho = rfl((u32)((float)(k.pm - k.qm) * 256.f * __builtin_amdgcn_rcpf((float)dr))); k.qr = k.pr; k.qm = k.pm; }
}
#define CLK_OUT [pr_] "+s"(k.pr), [pm_] "+s"(k.pm), [slot] "+s"(k.F), [tm] "+s"(tm)
#define CLK_IN [rho] "s"(k.rho), [q] TQc(TQv)
#define TRY_OUT [miss] "=&s"(miss), [n] "+s"(n)
#define TRY_IN [lim] "s"(lim)
#define SLK(s, w, T, T0) do {} while (0)
DI u32 mem_slot(const Ctx& c, u32 sg) { return (2 * sg) * 16384u + c.par0 * 8192u; }
DI u32 p_slot(const Ctx& c, u32 sg) { return (6 + 3 * sg) * 16384u + c.par0 * 8192u; }
DI u32 y_slot(const Ctx& c, u32 sg) { return sg == 2 ? 128u * 16384u + c.par1 * 8192u : (15 + 40 * sg) * 16384u + c.par0 * 8192u; }
constexpr u32 kb(int i) { return (u32)(i >> 3) * 16384u + (u32)(i & 7) * 1024u; }

#define EV(k) do {} while (0)
#define TSAVE(s, n) do {} while (0)
#define DUMP(slot) do {} while (0)
#define ST0
#define ST1
#define ST2
#define ST3
#define ST4
#define ST5
#define ST6
#define ST7
#define ST8
#define BX
#define STR0
#define STR1
#define STEND
#define STOPS
#define TSDECL
#define TS(i)
#define TS7
#define RST
#define WARMUP
#define FILL
#define FILL1 "s_nop 0\n\t"
#define CSL 2
#define STR2_(x) #x
#define STR2(x) STR2_(x)
#define CSLEEP "s_sleep " STR2(CSL) "\n\t"
#define A_BODY0 \
    "s_mov_b32 %[miss], 0\n\t" \
    "s_branch 0f\n\t" \
    "8:\n\t" \
    RST \
    "s_add_u32 %[n], %[n], 1\n\t" \
    "s_cmp_gt_u32 %[n], %[lim]\n\t" \
    "s_cselect_b32 %[miss], 1, 0\n\t" \
    "s_cbranch_scc1 9f\n\t" \
    "s_add_u32 %[slot], %[slot], %[q]\n\t" \
    "s_cmp_lg_u32 %[tm], 0\n\t" \
    "s_cselect_b32 %[tm], 2, 0\n\t" \
    "v_mov_b32 %[rpu], 0x80000000\n\t" \
    "v_mov_b32 %[rpm], 0x80000000\n\t" \
    "s_nop 7\n\t" \
    "s_waitcnt lgkmcnt(0)\n\t" \
    "0:\n\t" \
    "s_cmp_eq_u32 %[tm], 0\n\t" \
    "s_cbranch_scc1 5f\n\t" \
    "s_cmp_eq_u32 %[tm], 2\n\t" \
    "s_cbranch_scc1 3f\n\t" \
    "s_memtime s[60:61]\n\t" \
    "s_memrealtime s[64:65]\n\t" \
    "s_nop 0\n\t" \
    "s_memrealtime s[66:67]\n\t" \
    "s_nop 0\n\t" \
    "s_memrealtime s[68:69]\n\t" \
    "s_nop 0\n\t" \
    "s_memrealtime s[70:71]\n\t" \
    "s_nop 0\n\t" \
    "s_memrealtime s[72:73]\n\t" \
    "s_nop 0\n\t" \
    "s_memrealtime s[74:75]\n\t" \
    "s_nop 0\n\t" \
    "s_memrealtime s[76:77]\n\t" \
    "s_nop 0\n\t" \
    "s_memrealtime s[78:79]\n\t" \
    "s_nop 0\n\t" \
    "s_memrealtime s[80:81]\n\t" \
    "s_nop 0\n\t" \
    "s_memrealtime s[82:83]\n\t" \
    "s_nop 0\n\t" \
    "s_memrealtime s[84:85]\n\t" \
    "s_nop 0\n\t" \
    "s_memrealtime s[86:87]\n\t" \
    "s_nop 0\n\t" \
    "s_memrealtime s[88:89]\n\t" \
    "s_nop 0\n\t" \
    "s_memrealtime s[90:91]\n\t" \
    "s_nop 0\n\t" \
    "s_waitcnt lgkmcnt(0)\n\t" \
    "s_sub_u32 s61, s90, s64\n\t" \
    "s_add_u32 s64, s64, s66\n\t" \
    "s_add_u32 s64, s64, s68\n\t" \
    "s_add_u32 s64, s64, s70\n\t" \
    "s_add_u32 s64, s64, s72\n\t" \
    "s_add_u32 s64, s64, s74\n\t" \
    "s_add_u32 s64, s64, s76\n\t" \
    "s_add_u32 s64, s64, s78\n\t" \
    "s_add_u32 s64, s64, s80\n\t" \
    "s_add_u32 s64, s64, s82\n\t" \
    "s_add_u32 s64, s64, s84\n\t" \
    "s_add_u32 s64, s64, s86\n\t" \
    "s_add_u32 s64, s64, s88\n\t" \
    "s_mul_i32 s62, s90, 13\n\t" \
    "s_sub_u32 s62, s62, s64\n\t" \
    "s_cmp_eq_u32 s61, 4\n\t" \
    "s_cselect_b32 s63, s62, 47\n\t" \
    "s_add_u32 s62, s62, s63\n\t" \
    "s_or_b32 s63, s61, 12\n\t" \
    "s_cmp_lg_u32 s63, 12\n\t" \
    "s_cbranch_scc1 3f\n\t" \
    "s_cmp_eq_u32 s61, 0\n\t" \
    "s_cbranch_scc1 3f\n\t" \
    "s_add_u32 %[pm_], s62, s60\n\t" \
    "s_mov_b32 %[pr_], s90\n\t" \
    "3:\n\t" \
    "s_sub_u32 s60, %[slot], %[pr_]\n\t" \
    "s_mul_i32 s60, s60, %[rho]\n\t" \
    "s_ashr_i32 s60, s60, 8\n\t" \
    "s_add_u32 s62, s60, %[pm_]\n\t" \
    "4:\n\t" \
    "s_memtime vcc\n\t" \
    "s_waitcnt lgkmcnt(0)\n\t" \
    "s_sub_u32 vcc_lo, s62, vcc_lo\n\t" \
    "s_cmp_gt_i32 vcc_lo, 128\n\t" \
    "s_cbranch_scc1 4b\n\t" \
    "s_max_i32 vcc_lo, vcc_lo, 0\n\t" \
    "s_and_b32 vcc_lo, vcc_lo, -4\n\t" \
    "s_sub_u32 s62, 140, vcc_lo\n\t" \
    "s_getpc_b64 vcc\n\t" \
    "s_add_u32 vcc_lo, vcc_lo, s62\n\t" \
    "s_addc_u32 vcc_hi, vcc_hi, 0\n\t" \
    "s_setpc_b64 vcc\n\t" \
    ".rept 32\n\t" \
    "s_nop 0\n\t" \
    ".endr\n\t" \
    "5:\n\t" \
    "s_barrier\n\t" \
    "s_mov_b32 m0, %[l0]\n\t" \
    "s_nop 0\n\t" \
    "buffer_load_dwordx4 %[v0], %[hot], %[mb0] offen sc1 lds\n\t" \
    "s_mov_b32 m0, %[l1]\n\t" \
    "s_nop 0\n\t" \
    "buffer_load_dwordx4 %[v0], %[hot], %[mb1] offen sc1 lds\n\t" \
    "s_mov_b32 m0, %[l2]\n\t" \
    "s_nop 0\n\t" \
    "buffer_load_dwordx4 %[v2], %[hot], %[mb2] offen sc1 lds\n\t" \
    STR0 \
    ST0 \
    WARMUP \
    "v_mov_b64 v[216:217], 0\n\t" \
    "v_mov_b64 v[218:219], 0\n\t" \
    "v_mov_b64 v[220:221], 0\n\t" \
    "v_mov_b64 v[222:223], 0\n\t" \
    "v_mov_b64 v[224:225], 0\n\t" \
    "v_mov_b64 v[226:227], 0\n\t" \
    "v_mov_b64 v[228:229], 0\n\t" \
    "v_mov_b64 v[230:231], 0\n\t" \
    "v_mov_b64 v[250:251], -1\n\t" \
    "s_memtime s[60:61]\n\t" \
    "s_memrealtime s[64:65]\n\t" \
    "s_nop 0\n\t" \
    "s_memrealtime s[66:67]\n\t" \
    "s_nop 0\n\t" \
    "s_memrealtime s[68:69]\n\t" \
    "s_nop 0\n\t" \
    "s_memrealtime s[70:71]\n\t" \
    "s_nop 0\n\t" \
    "s_memrealtime s[72:73]\n\t" \
    "s_nop 0\n\t" \
    "s_memrealtime s[74:75]\n\t" \
    "s_nop 0\n\t" \
    "s_memrealtime s[76:77]\n\t" \
    "s_nop 0\n\t" \
    "s_memrealtime s[78:79]\n\t" \
    "s_nop 0\n\t" \
    "s_memrealtime s[80:81]\n\t" \
    "s_nop 0\n\t" \
    "s_memrealtime s[82:83]\n\t" \
    "s_nop 0\n\t" \
    "s_memrealtime s[84:85]\n\t" \
    "s_nop 0\n\t" \
    "s_memrealtime s[86:87]\n\t" \
    "s_nop 0\n\t" \
    "s_memrealtime s[88:89]\n\t" \
    "s_nop 0\n\t" \
    "s_memrealtime s[90:91]\n\t" \
    "s_nop 0\n\t" \
    "s_waitcnt lgkmcnt(0)\n\t" \
    "s_sub_u32 s61, s90, s64\n\t" \
    "s_add_u32 s64, s64, s66\n\t" \
    "s_add_u32 s64, s64, s68\n\t" \
    "s_add_u32 s64, s64, s70\n\t" \
    "s_add_u32 s64, s64, s72\n\t" \
    "s_add_u32 s64, s64, s74\n\t" \
    "s_add_u32 s64, s64, s76\n\t" \
    "s_add_u32 s64, s64, s78\n\t" \
    "s_add_u32 s64, s64, s80\n\t" \
    "s_add_u32 s64, s64, s82\n\t" \
    "s_add_u32 s64, s64, s84\n\t" \
    "s_add_u32 s64, s64, s86\n\t" \
    "s_add_u32 s64, s64, s88\n\t" \
    "s_mul_i32 s62, s90, 13\n\t" \
    "s_sub_u32 s62, s62, s64\n\t" \
    "s_cmp_eq_u32 s61, 4\n\t" \
    "s_cselect_b32 s63, s62, 47\n\t" \
    "s_add_u32 s62, s62, s63\n\t" \
    "s_or_b32 s63, s61, 12\n\t" \
    "s_cmp_lg_u32 s63, 12\n\t" \
    "s_cbranch_scc1 2f\n\t" \
    "s_cmp_eq_u32 s61, 0\n\t" \
    "s_cbranch_scc1 2f\n\t" \
    "s_add_u32 %[pm_], s62, s60\n\t" \
    "s_mov_b32 %[pr_], s90\n\t" \
    "2:\n\t" \
    "s_waitcnt vmcnt(2)\n\t" \
    "s_barrier\n\t" \
    "ds_read_b128 v[128:131], %[fa] offset:0\n\t" \
    "ds_read_b128 v[132:135], %[fa] offset:1024\n\t" \
    "ds_read_b128 v[136:139], %[fa] offset:2048\n\t" \
    "ds_read_b128 v[140:143], %[fa] offset:3072\n\t" \
    ST1 \
    "s_waitcnt lgkmcnt(3)\n\t" \
    "v_smfmac_f32_16x16x64_bf16 v[216:219], v[128:131], %[w00], %[idx]\n\t" \
    FILL \
    "v_smfmac_f32_16x16x64_bf16 v[220:223], v[128:131], %[w01], %[idx]\n\t" \
    FILL \
    "s_waitcnt lgkmcnt(2)\n\t" \
    "v_smfmac_f32_16x16x64_bf16 v[216:219], v[132:135], %[w10], %[idx]\n\t" \
    FILL \
    "v_smfmac_f32_16x16x64_bf16 v[220:223], v[132:135], %[w11], %[idx]\n\t" \
    FILL \
    "s_waitcnt vmcnt(0)\n\t" \
    "s_barrier\n\t" \
    "ds_read_b128 v[144:147], %[fa] offset:4096\n\t" \
    "ds_read_b128 v[148:151], %[fa] offset:5120\n\t" \
    "ds_read_b128 v[152:155], %[fa] offset:6144\n\t" \
    "ds_read_b128 v[156:159], %[fa] offset:7168\n\t" \
    "ds_read_b128 v[160:163], %[fa] offset:8192\n\t" \
    "ds_read_b128 v[164:167], %[fa] offset:9216\n\t" \
    ST2 \
    "s_waitcnt lgkmcnt(6)\n\t" \
    "v_smfmac_f32_16x16x64_bf16 v[216:219], v[136:139], %[w20], %[idx]\n\t" \
    "v_smfmac_f32_16x16x64_bf16 v[220:223], v[136:139], %[w21], %[idx]\n\t" \
    "v_smfmac_f32_16x16x64_bf16 v[216:219], v[140:143], %[w30], %[idx]\n\t" \
    "buffer_store_dwordx2 v[250:251], %[rpu], %[hot], %[rb] offen sc1\n\t" \
    "v_smfmac_f32_16x16x64_bf16 v[220:223], v[140:143], %[w31], %[idx]\n\t" \
    "s_waitcnt lgkmcnt(5)\n\t" \
    "v_smfmac_f32_16x16x64_bf16 v[216:219], v[144:147], %[w40], %[idx]\n\t" \
    "buffer_store_dword v250, %[rpm], %[hot], %[rb2] offen sc1\n\t" \
    "v_smfmac_f32_16x16x64_bf16 v[220:223], v[144:147], %[w41], %[idx]\n\t" \
    "s_waitcnt lgkmcnt(4)\n\t" \
    "v_smfmac_f32_16x16x64_bf16 v[216:219], v[148:151], %[w50], %[idx]\n\t" \
    "v_smfmac_f32_16x16x64_bf16 v[220:223], v[148:151], %[w51], %[idx]\n\t" \
    "s_waitcnt lgkmcnt(3)\n\t" \
    "v_smfmac_f32_16x16x64_bf16 v[216:219], v[152:155], %[w60], %[idx]\n\t" \
    "v_smfmac_f32_16x16x64_bf16 v[220:223], v[152:155], %[w61], %[idx]\n\t" \
    "s_waitcnt lgkmcnt(2)\n\t" \
    "v_smfmac_f32_16x16x64_bf16 v[216:219], v[156:159], %[w70], %[idx]\n\t" \
    "v_smfmac_f32_16x16x64_bf16 v[220:223], v[156:159], %[w71], %[idx]\n\t" \
    "s_waitcnt lgkmcnt(1)\n\t" \
    "v_smfmac_f32_16x16x64_bf16 v[216:219], v[160:163], %[w80], %[idx]\n\t" \
    "v_smfmac_f32_16x16x64_bf16 v[220:223], v[160:163], %[w81], %[idx]\n\t" \
    "s_waitcnt lgkmcnt(0)\n\t" \
    "v_smfmac_f32_16x16x64_bf16 v[216:219], v[164:167], %[w90], %[idx]\n\t" \
    "v_smfmac_f32_16x16x64_bf16 v[220:223], v[164:167], %[w91], %[idx]\n\t" \
    ST3 \
    "v_smfmac_f32_16x16x64_bf16 v[224:227], v[128:131], %[w02], %[idx]\n\t" \
    "s_nop 0\n\t" \
    "ds_read_b128 v[168:171], %[kq] offset:0\n\t" \
    "ds_read_b128 v[172:175], %[kq] offset:1024\n\t" \
    "v_smfmac_f32_16x16x64_bf16 v[224:227], v[132:135], %[w12], %[idx]\n\t" \
    "v_add_f32 v244, v216, v218\n\t" \
    "ds_read_b128 v[176:179], %[kq] offset:2048\n\t" \
    "ds_read_b128 v[180:183], %[kq] offset:3072\n\t" \
    "v_smfmac_f32_16x16x64_bf16 v[224:227], v[136:139], %[w22], %[idx]\n\t" \
    "v_add_f32 v245, v217, v219\n\t" \
    "ds_read_b128 v[184:187], %[kq] offset:4096\n\t" \
    "ds_read_b128 v[188:191], %[kq] offset:5120\n\t" \
    "v_smfmac_f32_16x16x64_bf16 v[224:227], v[140:143], %[w32], %[idx]\n\t" \
    "v_add_f32 v246, v220, v222\n\t" \
    "ds_read_b128 v[192:195], %[kq] offset:6144\n\t" \
    "ds_read_b128 v[196:199], %[kq] offset:7168\n\t" \
    "v_smfmac_f32_16x16x64_bf16 v[224:227], v[144:147], %[w42], %[idx]\n\t" \
    "v_add_f32 v247, v221, v223\n\t" \
    "v_smfmac_f32_16x16x64_bf16 v[224:227], v[148:151], %[w52], %[idx]\n\t" \
    "v_cvt_pk_bf16_f32 v248, v244, v245\n\t" \
    "ds_write_b32 %[qw], v248\n\t" \
    "v_smfmac_f32_16x16x64_bf16 v[224:227], v[152:155], %[w62], %[idx]\n\t" \
    "v_cvt_pk_bf16_f32 v249, v246, v247\n\t" \
    "ds_write_b32 %[qw], v249 offset:256\n\t" \
    "v_smfmac_f32_16x16x64_bf16 v[224:227], v[156:159], %[w72], %[idx]\n\t" \
    "v_cmp_u_f32 vcc, v244, v245\n\t" \
    "v_smfmac_f32_16x16x64_bf16 v[224:227], v[160:163], %[w82], %[idx]\n\t" \
    "s_nop 0\n\t" \
    "v_smfmac_f32_16x16x64_bf16 v[224:227], v[164:167], %[w92], %[idx]\n\t" \
    "s_andn2_b64 vcc, vcc, %[force]\n\t" \
    "v_smfmac_f32_16x16x64_bf16 v[228:231], v[128:131], %[w03], %[idx]\n\t" \
    "s_waitcnt lgkmcnt(0)\n\t" \
    "s_barrier\n\t" \
    "s_cbranch_scc1 8b\n\t" \
    ST4 \
    "v_smfmac_f32_16x16x64_bf16 v[228:231], v[132:135], %[w13], %[idx]\n\t" \
    "s_nop 0\n\t" \
    "ds_read_b64_tr_b16 v[200:201], %[qr] offset:0\n\t" \
    "ds_read_b64_tr_b16 v[202:203], %[qr] offset:256\n\t" \
    "ds_read_b64_tr_b16 v[204:205], %[qr] offset:512\n\t" \
    "ds_read_b64_tr_b16 v[206:207], %[qr] offset:768\n\t" \
    "ds_read_b64_tr_b16 v[208:209], %[qr] offset:1024\n\t" \
    "ds_read_b64_tr_b16 v[210:211], %[qr] offset:1280\n\t" \
    "ds_read_b64_tr_b16 v[212:213], %[qr] offset:1536\n\t" \
    "ds_read_b64_tr_b16 v[214:215], %[qr] offset:1792\n\t" \
    "v_smfmac_f32_16x16x64_bf16 v[228:231], v[136:139], %[w23], %[idx]\n\t" \
    "v_add_f32 v244, v224, v226\n\t" \
    "v_smfmac_f32_16x16x64_bf16 v[228:231], v[140:143], %[w33], %[idx]\n\t" \
    "v_add_f32 v245, v225, v227\n\t" \
    "v_smfmac_f32_16x16x64_bf16 v[228:231], v[144:147], %[w43], %[idx]\n\t" \
    "v_cvt_pk_bf16_f32 v248, v244, v245\n\t" \
    "ds_write_b32 %[kw], v248\n\t" \
    "v_smfmac_f32_16x16x64_bf16 v[228:231], v[148:151], %[w53], %[idx]\n\t" \
    FILL1 \
    "v_smfmac_f32_16x16x64_bf16 v[228:231], v[152:155], %[w63], %[idx]\n\t" \
    FILL1 \
    "v_smfmac_f32_16x16x64_bf16 v[228:231], v[156:159], %[w73], %[idx]\n\t" \
    FILL1 \
    "v_smfmac_f32_16x16x64_bf16 v[228:231], v[160:163], %[w83], %[idx]\n\t" \
    FILL1 \
    "v_smfmac_f32_16x16x64_bf16 v[228:231], v[164:167], %[w93], %[idx]\n\t" \
    ST5 \
    "s_waitcnt lgkmcnt(1)\n\t" \
    "v_mfma_f32_16x16x32_bf16 v[236:239], v[168:171], v[200:203], 0\n\t" \
    "v_mfma_f32_16x16x32_bf16 v[240:243], v[184:187], v[200:203], 0\n\t" \
    "s_nop 1\n\t" \
    "v_add_f32 v244, v228, v230\n\t" \
    "v_add_f32 v245, v229, v231\n\t" \
    "v_cvt_pk_bf16_f32 v248, v244, v245\n\t" \
    "ds_write_b32 %[kw], v248 offset:512\n\t" \
    "v_mfma_f32_16x16x32_bf16 v[236:239], v[172:175], v[204:207], v[236:239]\n\t" \
    "v_mfma_f32_16x16x32_bf16 v[240:243], v[188:191], v[204:207], v[240:243]\n\t" \
    "v_mfma_f32_16x16x32_bf16 v[236:239], v[176:179], v[208:211], v[236:239]\n\t" \
    "s_waitcnt lgkmcnt(0)\n\t" \
    "s_barrier\n\t" \
    "ds_read_b64_tr_b16 v[128:129], %[kr] offset:0\n\t" \
    "ds_read_b64_tr_b16 v[130:131], %[kr] offset:512\n\t" \
    "ds_read_b64_tr_b16 v[132:133], %[kr] offset:1024\n\t" \
    "ds_read_b64_tr_b16 v[134:135], %[kr] offset:1536\n\t" \
    "ds_read_b64_tr_b16 v[136:137], %[kr] offset:2048\n\t" \
    "ds_read_b64_tr_b16 v[138:139], %[kr] offset:2560\n\t" \
    "ds_read_b64_tr_b16 v[140:141], %[kr] offset:3072\n\t" \
    "ds_read_b64_tr_b16 v[142:143], %[kr] offset:3584\n\t" \
    ST6 \
    "v_mfma_f32_16x16x32_bf16 v[240:243], v[192:195], v[208:211], v[240:243]\n\t" \
    "v_mfma_f32_16x16x32_bf16 v[236:239], v[180:183], v[212:215], v[236:239]\n\t" \
    "v_mfma_f32_16x16x32_bf16 v[240:243], v[196:199], v[212:215], v[240:243]\n\t" \
    "s_waitcnt lgkmcnt(6)\n\t" \
    "v_mfma_f32_16x16x32_bf16 v[232:235], v[128:131], v[200:203], 0\n\t" \
    "s_waitcnt lgkmcnt(4)\n\t" \
    "v_mfma_f32_16x16x32_bf16 v[232:235], v[132:135], v[204:207], v[232:235]\n\t" \
    "s_waitcnt lgkmcnt(2)\n\t" \
    "v_mfma_f32_16x16x32_bf16 v[232:235], v[136:139], v[208:211], v[232:235]\n\t" \
    "v_cndmask_b32 v244, v236, v240, %[pm]\n\t" \
    "v_cndmask_b32 v245, v237, v241, %[pm]\n\t" \
    "s_waitcnt lgkmcnt(0)\n\t" \
    "v_mfma_f32_16x16x32_bf16 v[232:235], v[140:143], v[212:215], v[232:235]\n\t" \
    ST7 \
    "v_cndmask_b32 v246, v238, v242, %[pm]\n\t" \
    "v_cndmask_b32 v247, v239, v243, %[pm]\n\t" \
    "v_max_f32 v248, v244, v245\n\t" \
    "v_max3_f32 v248, v248, v246, v247\n\t" \
    "v_mul_f32 v244, %[sc], v244\n\t" \
    "v_mul_f32 v245, %[sc], v245\n\t" \
    "v_max_f32_dpp v248, v248, v248 row_ror:8 row_mask:0xf bank_mask:0xf\n\t" \
    "v_mul_f32 v246, %[sc], v246\n\t" \
    "v_mul_f32 v247, %[sc], v247\n\t" \
    "v_max3_f32 v248, v248, v232, v233\n\t" \
    "v_mov_b32 v249, v248\n\t" \
    "v_mul_f32 v250, %[sc], v232\n\t" \
    "v_mul_f32 v251, %[sc], v233\n\t" \
    "v_permlane16_swap_b32 v248, v249\n\t" \
    "v_max_f32 v248, v248, v249\n\t" \
    "v_mov_b32 v249, v248\n\t" \
    "s_nop 1\n\t" \
    "v_permlane32_swap_b32 v248, v249\n\t" \
    "v_max_f32 v248, v248, v249\n\t" \
    "v_fma_f32 v244, v248, -%[sc], v244\n\t" \
    "v_fma_f32 v245, v248, -%[sc], v245\n\t" \
    "v_fma_f32 v246, v248, -%[sc], v246\n\t" \
    "v_fma_f32 v247, v248, -%[sc], v247\n\t" \
    "v_fma_f32 v250, v248, -%[sc], v250\n\t" \
    "v_fma_f32 v251, v248, -%[sc], v251\n\t" \
    "v_exp_f32 v244, v244\n\t" \
    "v_exp_f32 v245, v245\n\t" \
    "v_exp_f32 v246, v246\n\t" \
    "v_exp_f32 v247, v247\n\t" \
    "v_add_f32 v248, v244, v245\n\t" \
    "v_add_f32 v249, v246, v247\n\t" \
    "v_add_f32 v248, v248, v249\n\t" \
    "v_exp_f32 v250, v250\n\t" \
    "v_exp_f32 v251, v251\n\t" \
    "v_add_f32_dpp v248, v248, v248 row_ror:8 row_mask:0xf bank_mask:0xf\n\t" \
    "v_add_f32 v249, v250, v251\n\t" \
    "v_add_f32 v248, v248, v249\n\t" \
    "v_mov_b32 v249, v248\n\t" \
    "s_nop 1\n\t" \
    "v_permlane16_swap_b32 v248, v249\n\t" \
    "v_add_f32 v248, v248, v249\n\t" \
    "v_mov_b32 v249, v248\n\t" \
    "s_nop 1\n\t" \
    "v_permlane32_swap_b32 v248, v249\n\t" \
    "v_add_f32 v248, v248, v249\n\t" \
    "v_rcp_f32 v248, v248\n\t" \
    "s_nop 0\n\t" \
    "v_mul_f32 v244, v244, v248\n\t" \
    "v_mul_f32 v245, v245, v248\n\t" \
    "v_mul_f32 v246, v246, v248\n\t" \
    "v_mul_f32 v247, v247, v248\n\t" \
    "v_cvt_pk_bf16_f32 v252, v244, v245\n\t" \
    "v_cvt_pk_bf16_f32 v253, v246, v247\n\t" \
    "buffer_store_dwordx2 v[252:253], %[puo], %[hot], %[pb] offen sc1\n\t" \
    "v_mul_f32 v250, v250, v248\n\t" \
    "v_mul_f32 v251, v251, v248\n\t" \
    "v_cvt_pk_bf16_f32 v254, v250, v251\n\t" \
    "buffer_store_dword v254, %[pmo], %[hot], %[pb2] offen sc1\n\t" \
    ST8 \
    STR1 \
    "9:\n\t" \
    "s_waitcnt lgkmcnt(0)\n\t" \
    ""
#define A_BODY1 \
    "s_mov_b32 %[miss], 0\n\t" \
    "s_branch 0f\n\t" \
    "8:\n\t" \
    RST \
    "s_add_u32 %[n], %[n], 1\n\t" \
    "s_cmp_gt_u32 %[n], %[lim]\n\t" \
    "s_cselect_b32 %[miss], 1, 0\n\t" \
    "s_cbranch_scc1 9f\n\t" \
    "s_nop 7\n\t" \
    "s_waitcnt lgkmcnt(0)\n\t" \
    "0:\n\t" \
    "s_barrier\n\t" \
    "s_mov_b32 m0, %[l0]\n\t" \
    "s_nop 0\n\t" \
    "buffer_load_dwordx4 %[v0], %[hot], %[mb0] offen sc1 lds\n\t" \
    "s_mov_b32 m0, %[l1]\n\t" \
    "s_nop 0\n\t" \
    "buffer_load_dwordx4 %[v0], %[hot], %[mb1] offen sc1 lds\n\t" \
    "s_mov_b32 m0, %[l2]\n\t" \
    "s_nop 0\n\t" \
    "buffer_load_dwordx4 %[v2], %[hot], %[mb2] offen sc1 lds\n\t" \
    STR0 \
    ST0 \
    WARMUP \
    "v_mov_b64 v[216:217], 0\n\t" \
    "v_mov_b64 v[218:219], 0\n\t" \
    "v_mov_b64 v[220:221], 0\n\t" \
    "v_mov_b64 v[222:223], 0\n\t" \
    "v_mov_b64 v[224:225], 0\n\t" \
    "v_mov_b64 v[226:227], 0\n\t" \
    "v_mov_b64 v[228:229], 0\n\t" \
    "v_mov_b64 v[230:231], 0\n\t" \
    "s_waitcnt vmcnt(2)\n\t" \
    "s_barrier\n\t" \
    "ds_read_b128 v[128:131], %[fa] offset:0\n\t" \
    "ds_read_b128 v[132:135], %[fa] offset:1024\n\t" \
    "ds_read_b128 v[136:139], %[fa] offset:2048\n\t" \
    "ds_read_b128 v[140:143], %[fa] offset:3072\n\t" \
    ST1 \
    "s_waitcnt lgkmcnt(3)\n\t" \
    "v_smfmac_f32_16x16x64_bf16 v[216:219], v[128:131], %[w00], %[idx]\n\t" \
    FILL \
    "v_smfmac_f32_16x16x64_bf16 v[220:223], v[128:131], %[w01], %[idx]\n\t" \
    FILL \
    "s_waitcnt lgkmcnt(2)\n\t" \
    "v_smfmac_f32_16x16x64_bf16 v[216:219], v[132:135], %[w10], %[idx]\n\t" \
    FILL \
    "v_smfmac_f32_16x16x64_bf16 v[220:223], v[132:135], %[w11], %[idx]\n\t" \
    FILL \
    "s_waitcnt vmcnt(0)\n\t" \
    "s_barrier\n\t" \
    "ds_read_b128 v[144:147], %[fa] offset:4096\n\t" \
    "ds_read_b128 v[148:151], %[fa] offset:5120\n\t" \
    "ds_read_b128 v[152:155], %[fa] offset:6144\n\t" \
    "ds_read_b128 v[156:159], %[fa] offset:7168\n\t" \
    "ds_read_b128 v[160:163], %[fa] offset:8192\n\t" \
    "ds_read_b128 v[164:167], %[fa] offset:9216\n\t" \
    ST2 \
    "s_waitcnt lgkmcnt(6)\n\t" \
    "v_smfmac_f32_16x16x64_bf16 v[216:219], v[136:139], %[w20], %[idx]\n\t" \
    "v_smfmac_f32_16x16x64_bf16 v[220:223], v[136:139], %[w21], %[idx]\n\t" \
    "v_smfmac_f32_16x16x64_bf16 v[216:219], v[140:143], %[w30], %[idx]\n\t" \
    "v_smfmac_f32_16x16x64_bf16 v[220:223], v[140:143], %[w31], %[idx]\n\t" \
    "s_waitcnt lgkmcnt(5)\n\t" \
    "v_smfmac_f32_16x16x64_bf16 v[216:219], v[144:147], %[w40], %[idx]\n\t" \
    "v_smfmac_f32_16x16x64_bf16 v[220:223], v[144:147], %[w41], %[idx]\n\t" \
    "s_waitcnt lgkmcnt(4)\n\t" \
    "v_smfmac_f32_16x16x64_bf16 v[216:219], v[148:151], %[w50], %[idx]\n\t" \
    "v_smfmac_f32_16x16x64_bf16 v[220:223], v[148:151], %[w51], %[idx]\n\t" \
    "s_waitcnt lgkmcnt(3)\n\t" \
    "v_smfmac_f32_16x16x64_bf16 v[216:219], v[152:155], %[w60], %[idx]\n\t" \
    "v_smfmac_f32_16x16x64_bf16 v[220:223], v[152:155], %[w61], %[idx]\n\t" \
    "s_waitcnt lgkmcnt(2)\n\t" \
    "v_smfmac_f32_16x16x64_bf16 v[216:219], v[156:159], %[w70], %[idx]\n\t" \
    "v_smfmac_f32_16x16x64_bf16 v[220:223], v[156:159], %[w71], %[idx]\n\t" \
    "s_waitcnt lgkmcnt(1)\n\t" \
    "v_smfmac_f32_16x16x64_bf16 v[216:219], v[160:163], %[w80], %[idx]\n\t" \
    "v_smfmac_f32_16x16x64_bf16 v[220:223], v[160:163], %[w81], %[idx]\n\t" \
    "s_waitcnt lgkmcnt(0)\n\t" \
    "v_smfmac_f32_16x16x64_bf16 v[216:219], v[164:167], %[w90], %[idx]\n\t" \
    "v_smfmac_f32_16x16x64_bf16 v[220:223], v[164:167], %[w91], %[idx]\n\t" \
    ST3 \
    "v_smfmac_f32_16x16x64_bf16 v[224:227], v[128:131], %[w02], %[idx]\n\t" \
    "s_nop 0\n\t" \
    "v_smfmac_f32_16x16x64_bf16 v[224:227], v[132:135], %[w12], %[idx]\n\t" \
    "v_add_f32 v244, v216, v218\n\t" \
    "v_smfmac_f32_16x16x64_bf16 v[224:227], v[136:139], %[w22], %[idx]\n\t" \
    "v_add_f32 v245, v217, v219\n\t" \
    "v_smfmac_f32_16x16x64_bf16 v[224:227], v[140:143], %[w32], %[idx]\n\t" \
    "v_add_f32 v246, v220, v222\n\t" \
    "v_smfmac_f32_16x16x64_bf16 v[224:227], v[144:147], %[w42], %[idx]\n\t" \
    "v_add_f32 v247, v221, v223\n\t" \
    "v_smfmac_f32_16x16x64_bf16 v[224:227], v[148:151], %[w52], %[idx]\n\t" \
    "v_cvt_pk_bf16_f32 v248, v244, v245\n\t" \
    "ds_write_b32 %[qw], v248\n\t" \
    "v_smfmac_f32_16x16x64_bf16 v[224:227], v[152:155], %[w62], %[idx]\n\t" \
    "v_cvt_pk_bf16_f32 v249, v246, v247\n\t" \
    "ds_write_b32 %[qw], v249 offset:256\n\t" \
    "v_smfmac_f32_16x16x64_bf16 v[224:227], v[156:159], %[w72], %[idx]\n\t" \
    "v_cmp_u_f32 vcc, v244, v245\n\t" \
    "v_smfmac_f32_16x16x64_bf16 v[224:227], v[160:163], %[w82], %[idx]\n\t" \
    "s_nop 0\n\t" \
    "v_smfmac_f32_16x16x64_bf16 v[224:227], v[164:167], %[w92], %[idx]\n\t" \
    "s_andn2_b64 vcc, vcc, %[force]\n\t" \
    "v_smfmac_f32_16x16x64_bf16 v[228:231], v[128:131], %[w03], %[idx]\n\t" \
    "s_waitcnt lgkmcnt(0)\n\t" \
    "s_barrier\n\t" \
    "s_cbranch_scc1 8b\n\t" \
    ST4 \
    "v_smfmac_f32_16x16x64_bf16 v[228:231], v[132:135], %[w13], %[idx]\n\t" \
    "s_nop 0\n\t" \
    "v_smfmac_f32_16x16x64_bf16 v[228:231], v[136:139], %[w23], %[idx]\n\t" \
    "v_add_f32 v244, v224, v226\n\t" \
    "v_smfmac_f32_16x16x64_bf16 v[228:231], v[140:143], %[w33], %[idx]\n\t" \
    "v_add_f32 v245, v225, v227\n\t" \
    "v_smfmac_f32_16x16x64_bf16 v[228:231], v[144:147], %[w43], %[idx]\n\t" \
    "v_cvt_pk_bf16_f32 v248, v244, v245\n\t" \
    "ds_write_b32 %[kw], v248\n\t" \
    "v_smfmac_f32_16x16x64_bf16 v[228:231], v[148:151], %[w53], %[idx]\n\t" \
    FILL1 \
    "v_smfmac_f32_16x16x64_bf16 v[228:231], v[152:155], %[w63], %[idx]\n\t" \
    FILL1 \
    "v_smfmac_f32_16x16x64_bf16 v[228:231], v[156:159], %[w73], %[idx]\n\t" \
    FILL1 \
    "v_smfmac_f32_16x16x64_bf16 v[228:231], v[160:163], %[w83], %[idx]\n\t" \
    FILL1 \
    "v_smfmac_f32_16x16x64_bf16 v[228:231], v[164:167], %[w93], %[idx]\n\t" \
    ST5 \
    "s_nop 7\n\t" \
    "v_add_f32 v244, v228, v230\n\t" \
    "v_add_f32 v245, v229, v231\n\t" \
    "v_cvt_pk_bf16_f32 v248, v244, v245\n\t" \
    "ds_write_b32 %[kw], v248 offset:512\n\t" \
    "s_waitcnt lgkmcnt(0)\n\t" \
    "s_barrier\n\t" \
    "9:\n\t" \
    "s_waitcnt lgkmcnt(0)\n\t" \
    ""
#define A_BODY3 \
    "s_mov_b32 %[miss], 0\n\t" \
    "s_branch 0f\n\t" \
    "8:\n\t" \
    RST \
    "s_add_u32 %[n], %[n], 1\n\t" \
    "s_cmp_gt_u32 %[n], %[lim]\n\t" \
    "s_cselect_b32 %[miss], 1, 0\n\t" \
    "s_cbranch_scc1 9f\n\t" \
    "v_mov_b32 %[dv], 0x80000000\n\t" \
    "s_mov_b32 %[dl], %[junk]\n\t" \
    "s_nop 7\n\t" \
    "s_waitcnt lgkmcnt(0)\n\t" \
    "0:\n\t" \
    "s_barrier\n\t" \
    "s_mov_b32 m0, %[l0]\n\t" \
    "s_nop 0\n\t" \
    "buffer_load_dwordx4 %[v0], %[hot], %[mb0] offen sc1 lds\n\t" \
    "s_mov_b32 m0, %[l1]\n\t" \
    "s_nop 0\n\t" \
    "buffer_load_dwordx4 %[v0], %[hot], %[mb1] offen sc1 lds\n\t" \
    "s_mov_b32 m0, %[l2]\n\t" \
    "s_nop 0\n\t" \
    "buffer_load_dwordx4 %[v2], %[hot], %[mb2] offen sc1 lds\n\t" \
    STR0 \
    ST0 \
    WARMUP \
    "v_mov_b64 v[216:217], 0\n\t" \
    "v_mov_b64 v[218:219], 0\n\t" \
    "v_mov_b64 v[220:221], 0\n\t" \
    "v_mov_b64 v[222:223], 0\n\t" \
    "v_mov_b64 v[224:225], 0\n\t" \
    "v_mov_b64 v[226:227], 0\n\t" \
    "v_mov_b64 v[228:229], 0\n\t" \
    "v_mov_b64 v[230:231], 0\n\t" \
    "s_waitcnt vmcnt(2)\n\t" \
    "s_barrier\n\t" \
    "ds_read_b128 v[128:131], %[fa] offset:0\n\t" \
    "ds_read_b128 v[132:135], %[fa] offset:1024\n\t" \
    "ds_read_b128 v[136:139], %[fa] offset:2048\n\t" \
    "ds_read_b128 v[140:143], %[fa] offset:3072\n\t" \
    ST1 \
    "s_waitcnt lgkmcnt(3)\n\t" \
    "v_smfmac_f32_16x16x64_bf16 v[216:219], v[128:131], %[w00], %[idx]\n\t" \
    FILL \
    "v_smfmac_f32_16x16x64_bf16 v[220:223], v[128:131], %[w01], %[idx]\n\t" \
    FILL \
    "s_waitcnt lgkmcnt(2)\n\t" \
    "v_smfmac_f32_16x16x64_bf16 v[216:219], v[132:135], %[w10], %[idx]\n\t" \
    FILL \
    "v_smfmac_f32_16x16x64_bf16 v[220:223], v[132:135], %[w11], %[idx]\n\t" \
    FILL \
    "s_waitcnt vmcnt(0)\n\t" \
    "s_barrier\n\t" \
    "ds_read_b128 v[144:147], %[fa] offset:4096\n\t" \
    "ds_read_b128 v[148:151], %[fa] offset:5120\n\t" \
    "ds_read_b128 v[152:155], %[fa] offset:6144\n\t" \
    "ds_read_b128 v[156:159], %[fa] offset:7168\n\t" \
    "ds_read_b128 v[160:163], %[fa] offset:8192\n\t" \
    "ds_read_b128 v[164:167], %[fa] offset:9216\n\t" \
    ST2 \
    "s_mov_b32 m0, %[dl]\n\t" \
    "s_mov_b32 s60, %[ds]\n\t" \
    "s_waitcnt lgkmcnt(6)\n\t" \
    "v_smfmac_f32_16x16x64_bf16 v[216:219], v[136:139], %[w20], %[idx]\n\t" \
    "buffer_load_dwordx4 %[dv], %[big], s60 offen offset:0 sc1 lds\n\t" \
    "v_smfmac_f32_16x16x64_bf16 v[220:223], v[136:139], %[w21], %[idx]\n\t" \
    "buffer_load_dwordx4 %[dv], %[big], s60 offen offset:1024 sc1 lds\n\t" \
    "v_smfmac_f32_16x16x64_bf16 v[216:219], v[140:143], %[w30], %[idx]\n\t" \
    "buffer_load_dwordx4 %[dv], %[big], s60 offen offset:2048 sc1 lds\n\t" \
    "v_smfmac_f32_16x16x64_bf16 v[220:223], v[140:143], %[w31], %[idx]\n\t" \
    "buffer_load_dwordx4 %[dv], %[big], s60 offen offset:3072 sc1 lds\n\t" \
    "s_add_u32 m0, m0, 4096\n\t" \
    "s_add_u32 s60, s60, 4096\n\t" \
    "s_waitcnt lgkmcnt(5)\n\t" \
    "v_smfmac_f32_16x16x64_bf16 v[216:219], v[144:147], %[w40], %[idx]\n\t" \
    "buffer_load_dwordx4 %[dv], %[big], s60 offen offset:0 sc1 lds\n\t" \
    "v_smfmac_f32_16x16x64_bf16 v[220:223], v[144:147], %[w41], %[idx]\n\t" \
    "buffer_load_dwordx4 %[dv], %[big], s60 offen offset:1024 sc1 lds\n\t" \
    "s_waitcnt lgkmcnt(4)\n\t" \
    "v_smfmac_f32_16x16x64_bf16 v[216:219], v[148:151], %[w50], %[idx]\n\t" \
    "buffer_load_dwordx4 %[dv], %[big], s60 offen offset:2048 sc1 lds\n\t" \
    "v_smfmac_f32_16x16x64_bf16 v[220:223], v[148:151], %[w51], %[idx]\n\t" \
    "buffer_load_dwordx4 %[dv], %[big], s60 offen offset:3072 sc1 lds\n\t" \
    "s_add_u32 m0, m0, 4096\n\t" \
    "s_add_u32 s60, s60, 4096\n\t" \
    "s_waitcnt lgkmcnt(3)\n\t" \
    "v_smfmac_f32_16x16x64_bf16 v[216:219], v[152:155], %[w60], %[idx]\n\t" \
    "buffer_load_dwordx4 %[dv], %[big], s60 offen offset:0 sc1 lds\n\t" \
    "v_smfmac_f32_16x16x64_bf16 v[220:223], v[152:155], %[w61], %[idx]\n\t" \
    "buffer_load_dwordx4 %[dv], %[big], s60 offen offset:1024 sc1 lds\n\t" \
    "s_waitcnt lgkmcnt(2)\n\t" \
    "v_smfmac_f32_16x16x64_bf16 v[216:219], v[156:159], %[w70], %[idx]\n\t" \
    "buffer_load_dwordx4 %[dv], %[big], s60 offen offset:2048 sc1 lds\n\t" \
    "v_smfmac_f32_16x16x64_bf16 v[220:223], v[156:159], %[w71], %[idx]\n\t" \
    "buffer_load_dwordx4 %[dv], %[big], s60 offen offset:3072 sc1 lds\n\t" \
    "s_add_u32 m0, m0, 4096\n\t" \
    "s_add_u32 s60, s60, 4096\n\t" \
    "s_waitcnt lgkmcnt(1)\n\t" \
    "v_smfmac_f32_16x16x64_bf16 v[216:219], v[160:163], %[w80], %[idx]\n\t" \
    "buffer_load_dwordx4 %[dv], %[big], s60 offen offset:0 sc1 lds\n\t" \
    "v_smfmac_f32_16x16x64_bf16 v[220:223], v[160:163], %[w81], %[idx]\n\t" \
    "buffer_load_dwordx4 %[dv], %[big], s60 offen offset:1024 sc1 lds\n\t" \
    "s_waitcnt lgkmcnt(0)\n\t" \
    "v_smfmac_f32_16x16x64_bf16 v[216:219], v[164:167], %[w90], %[idx]\n\t" \
    "buffer_load_dwordx4 %[dv], %[big], s60 offen offset:2048 sc1 lds\n\t" \
    "v_smfmac_f32_16x16x64_bf16 v[220:223], v[164:167], %[w91], %[idx]\n\t" \
    "buffer_load_dwordx4 %[dv], %[big], s60 offen offset:3072 sc1 lds\n\t" \
    ST3 \
    "v_smfmac_f32_16x16x64_bf16 v[224:227], v[128:131], %[w02], %[idx]\n\t" \
    "s_nop 0\n\t" \
    "v_smfmac_f32_16x16x64_bf16 v[224:227], v[132:135], %[w12], %[idx]\n\t" \
    "v_add_f32 v244, v216, v218\n\t" \
    "v_smfmac_f32_16x16x64_bf16 v[224:227], v[136:139], %[w22], %[idx]\n\t" \
    "v_add_f32 v245, v217, v219\n\t" \
    "v_smfmac_f32_16x16x64_bf16 v[224:227], v[140:143], %[w32], %[idx]\n\t" \
    "v_add_f32 v246, v220, v222\n\t" \
    "v_smfmac_f32_16x16x64_bf16 v[224:227], v[144:147], %[w42], %[idx]\n\t" \
    "v_add_f32 v247, v221, v223\n\t" \
    "v_smfmac_f32_16x16x64_bf16 v[224:227], v[148:151], %[w52], %[idx]\n\t" \
    "v_cvt_pk_bf16_f32 v248, v244, v245\n\t" \
    "ds_write_b32 %[qw], v248\n\t" \
    "v_smfmac_f32_16x16x64_bf16 v[224:227], v[152:155], %[w62], %[idx]\n\t" \
    "v_cvt_pk_bf16_f32 v249, v246, v247\n\t" \
    "ds_write_b32 %[qw], v249 offset:256\n\t" \
    "v_smfmac_f32_16x16x64_bf16 v[224:227], v[156:159], %[w72], %[idx]\n\t" \
    "v_cmp_u_f32 vcc, v244, v245\n\t" \
    "v_smfmac_f32_16x16x64_bf16 v[224:227], v[160:163], %[w82], %[idx]\n\t" \
    "s_nop 0\n\t" \
    "v_smfmac_f32_16x16x64_bf16 v[224:227], v[164:167], %[w92], %[idx]\n\t" \
    "s_andn2_b64 vcc, vcc, %[force]\n\t" \
    "v_smfmac_f32_16x16x64_bf16 v[228:231], v[128:131], %[w03], %[idx]\n\t" \
    "s_waitcnt lgkmcnt(0)\n\t" \
    "s_barrier\n\t" \
    "s_cbranch_scc1 8b\n\t" \
    ST4 \
    "v_smfmac_f32_16x16x64_bf16 v[228:231], v[132:135], %[w13], %[idx]\n\t" \
    "s_nop 0\n\t" \
    "v_smfmac_f32_16x16x64_bf16 v[228:231], v[136:139], %[w23], %[idx]\n\t" \
    "v_add_f32 v244, v224, v226\n\t" \
    "v_smfmac_f32_16x16x64_bf16 v[228:231], v[140:143], %[w33], %[idx]\n\t" \
    "v_add_f32 v245, v225, v227\n\t" \
    "v_smfmac_f32_16x16x64_bf16 v[228:231], v[144:147], %[w43], %[idx]\n\t" \
    "v_cvt_pk_bf16_f32 v248, v244, v245\n\t" \
    "ds_write_b32 %[kw], v248\n\t" \
    "v_smfmac_f32_16x16x64_bf16 v[228:231], v[148:151], %[w53], %[idx]\n\t" \
    FILL1 \
    "v_smfmac_f32_16x16x64_bf16 v[228:231], v[152:155], %[w63], %[idx]\n\t" \
    FILL1 \
    "v_smfmac_f32_16x16x64_bf16 v[228:231], v[156:159], %[w73], %[idx]\n\t" \
    FILL1 \
    "v_smfmac_f32_16x16x64_bf16 v[228:231], v[160:163], %[w83], %[idx]\n\t" \
    FILL1 \
    "v_smfmac_f32_16x16x64_bf16 v[228:231], v[164:167], %[w93], %[idx]\n\t" \
    ST5 \
    "s_nop 7\n\t" \
    "v_add_f32 v244, v228, v230\n\t" \
    "v_add_f32 v245, v229, v231\n\t" \
    "v_cvt_pk_bf16_f32 v248, v244, v245\n\t" \
    "ds_write_b32 %[kw], v248 offset:512\n\t" \
    "s_waitcnt lgkmcnt(0)\n\t" \
    "s_barrier\n\t" \
    "9:\n\t" \
    "s_waitcnt lgkmcnt(0)\n\t" \
    ""
#define C_BODY \
    "s_mov_b32 %[miss], 0\n\t" \
    "s_branch 0f\n\t" \
    "8:\n\t" \
    RST \
    "s_add_u32 %[n], %[n], 1\n\t" \
    "s_cmp_gt_u32 %[n], %[lim]\n\t" \
    "s_cselect_b32 %[miss], 1, 0\n\t" \
    "s_cbranch_scc1 9f\n\t" \
    "s_add_u32 %[slot], %[slot], %[q]\n\t" \
    "s_cmp_lg_u32 %[tm], 0\n\t" \
    "s_cselect_b32 %[tm], 2, 0\n\t" \
    "v_mov_b32 %[ro], 0x80000000\n\t" \
    "v_mov_b32 %[ro2], 0x80000000\n\t" \
    "v_mov_b32 %[y2o], 0x80000000\n\t" \
    "v_mov_b32 %[y2o2], 0x80000000\n\t" \
    "s_nop 7\n\t" \
    "s_waitcnt lgkmcnt(0)\n\t" \
    "0:\n\t" \
    "s_cmp_eq_u32 %[tm], 0\n\t" \
    "s_cbranch_scc1 5f\n\t" \
    "s_cmp_eq_u32 %[tm], 2\n\t" \
    "s_cbranch_scc1 3f\n\t" \
    "s_memtime s[60:61]\n\t" \
    "s_memrealtime s[64:65]\n\t" \
    "s_nop 0\n\t" \
    "s_memrealtime s[66:67]\n\t" \
    "s_nop 0\n\t" \
    "s_memrealtime s[68:69]\n\t" \
    "s_nop 0\n\t" \
    "s_memrealtime s[70:71]\n\t" \
    "s_nop 0\n\t" \
    "s_memrealtime s[72:73]\n\t" \
    "s_nop 0\n\t" \
    "s_memrealtime s[74:75]\n\t" \
    "s_nop 0\n\t" \
    "s_memrealtime s[76:77]\n\t" \
    "s_nop 0\n\t" \
    "s_memrealtime s[78:79]\n\t" \
    "s_nop 0\n\t" \
    "s_memrealtime s[80:81]\n\t" \
    "s_nop 0\n\t" \
    "s_memrealtime s[82:83]\n\t" \
    "s_nop 0\n\t" \
    "s_memrealtime s[84:85]\n\t" \
    "s_nop 0\n\t" \
    "s_memrealtime s[86:87]\n\t" \
    "s_nop 0\n\t" \
    "s_memrealtime s[88:89]\n\t" \
    "s_nop 0\n\t" \
    "s_memrealtime s[90:91]\n\t" \
    "s_nop 0\n\t" \
    "s_waitcnt lgkmcnt(0)\n\t" \
    "s_sub_u32 s61, s90, s64\n\t" \
    "s_add_u32 s64, s64, s66\n\t" \
    "s_add_u32 s64, s64, s68\n\t" \
    "s_add_u32 s64, s64, s70\n\t" \
    "s_add_u32 s64, s64, s72\n\t" \
    "s_add_u32 s64, s64, s74\n\t" \
    "s_add_u32 s64, s64, s76\n\t" \
    "s_add_u32 s64, s64, s78\n\t" \
    "s_add_u32 s64, s64, s80\n\t" \
    "s_add_u32 s64, s64, s82\n\t" \
    "s_add_u32 s64, s64, s84\n\t" \
    "s_add_u32 s64, s64, s86\n\t" \
    "s_add_u32 s64, s64, s88\n\t" \
    "s_mul_i32 s62, s90, 13\n\t" \
    "s_sub_u32 s62, s62, s64\n\t" \
    "s_cmp_eq_u32 s61, 4\n\t" \
    "s_cselect_b32 s63, s62, 47\n\t" \
    "s_add_u32 s62, s62, s63\n\t" \
    "s_or_b32 s63, s61, 12\n\t" \
    "s_cmp_lg_u32 s63, 12\n\t" \
    "s_cbranch_scc1 3f\n\t" \
    "s_cmp_eq_u32 s61, 0\n\t" \
    "s_cbranch_scc1 3f\n\t" \
    "s_add_u32 %[pm_], s62, s60\n\t" \
    "s_mov_b32 %[pr_], s90\n\t" \
    "3:\n\t" \
    "s_sub_u32 s60, %[slot], %[pr_]\n\t" \
    "s_mul_i32 s60, s60, %[rho]\n\t" \
    "s_ashr_i32 s60, s60, 8\n\t" \
    "s_add_u32 s62, s60, %[pm_]\n\t" \
    "4:\n\t" \
    "s_memtime vcc\n\t" \
    "s_waitcnt lgkmcnt(0)\n\t" \
    "s_sub_u32 vcc_lo, s62, vcc_lo\n\t" \
    "s_cmp_gt_i32 vcc_lo, 128\n\t" \
    "s_cbranch_scc1 4b\n\t" \
    "s_max_i32 vcc_lo, vcc_lo, 0\n\t" \
    "s_and_b32 vcc_lo, vcc_lo, -4\n\t" \
    "s_sub_u32 s62, 140, vcc_lo\n\t" \
    "s_getpc_b64 vcc\n\t" \
    "s_add_u32 vcc_lo, vcc_lo, s62\n\t" \
    "s_addc_u32 vcc_hi, vcc_hi, 0\n\t" \
    "s_setpc_b64 vcc\n\t" \
    ".rept 32\n\t" \
    "s_nop 0\n\t" \
    ".endr\n\t" \
    "5:\n\t" \
    "s_barrier\n\t" \
    "s_mov_b32 m0, %[l0]\n\t" \
    "s_nop 0\n\t" \
    "buffer_load_dwordx4 %[v0], %[hot], %[mb0] offen sc1 lds\n\t" \
    "s_mov_b32 m0, %[l1]\n\t" \
    "s_nop 0\n\t" \
    "buffer_load_dwordx4 %[v0], %[hot], %[mb1] offen sc1 lds\n\t" \
    "s_mov_b32 m0, %[l2]\n\t" \
    "s_nop 0\n\t" \
    "buffer_load_dwordx4 %[v2], %[hot], %[mb2] offen sc1 lds\n\t" \
    STR0 \
    ST0 \
    WARMUP \
    "v_mov_b64 v[168:169], 0\n\t" \
    "v_mov_b64 v[170:171], 0\n\t" \
    "v_mov_b64 v[172:173], 0\n\t" \
    "v_mov_b64 v[174:175], 0\n\t" \
    "v_mov_b64 v[184:185], 0\n\t" \
    "v_mov_b64 v[186:187], 0\n\t" \
    "v_mov_b64 v[188:189], 0\n\t" \
    "v_mov_b64 v[190:191], 0\n\t" \
    "v_mov_b64 v[192:193], 0\n\t" \
    "v_mov_b64 v[194:195], 0\n\t" \
    "v_mov_b64 v[196:197], 0\n\t" \
    "v_mov_b64 v[198:199], 0\n\t" \
    "v_mov_b64 v[200:201], 0\n\t" \
    "v_mov_b64 v[202:203], 0\n\t" \
    "v_mov_b64 v[204:205], 0\n\t" \
    "v_mov_b64 v[206:207], 0\n\t" \
    "v_mov_b64 v[208:209], 0\n\t" \
    "v_mov_b64 v[210:211], 0\n\t" \
    "v_mov_b64 v[212:213], 0\n\t" \
    "v_mov_b64 v[214:215], 0\n\t" \
    "v_mov_b64 v[216:217], 0\n\t" \
    "v_mov_b64 v[218:219], 0\n\t" \
    "v_mov_b64 v[220:221], 0\n\t" \
    "v_mov_b64 v[222:223], 0\n\t" \
    "v_mov_b64 v[240:241], -1\n\t" \
    "v_mov_b64 v[242:243], -1\n\t" \
    "s_cmp_eq_u32 %[tm], 0\n\t" \
    "s_cbranch_scc1 2f\n\t" \
    "s_memtime s[60:61]\n\t" \
    "s_memrealtime s[64:65]\n\t" \
    "s_nop 0\n\t" \
    "s_memrealtime s[66:67]\n\t" \
    "s_nop 0\n\t" \
    "s_memrealtime s[68:69]\n\t" \
    "s_nop 0\n\t" \
    "s_memrealtime s[70:71]\n\t" \
    "s_nop 0\n\t" \
    "s_memrealtime s[72:73]\n\t" \
    "s_nop 0\n\t" \
    "s_memrealtime s[74:75]\n\t" \
    "s_nop 0\n\t" \
    "s_memrealtime s[76:77]\n\t" \
    "s_nop 0\n\t" \
    "s_memrealtime s[78:79]\n\t" \
    "s_nop 0\n\t" \
    "s_memrealtime s[80:81]\n\t" \
    "s_nop 0\n\t" \
    "s_memrealtime s[82:83]\n\t" \
    "s_nop 0\n\t" \
    "s_memrealtime s[84:85]\n\t" \
    "s_nop 0\n\t" \
    "s_memrealtime s[86:87]\n\t" \
    "s_nop 0\n\t" \
    "s_memrealtime s[88:89]\n\t" \
    "s_nop 0\n\t" \
    "s_memrealtime s[90:91]\n\t" \
    "s_nop 0\n\t" \
    "s_waitcnt lgkmcnt(0)\n\t" \
    "s_sub_u32 s61, s90, s64\n\t" \
    "s_add_u32 s64, s64, s66\n\t" \
    "s_add_u32 s64, s64, s68\n\t" \
    "s_add_u32 s64, s64, s70\n\t" \
    "s_add_u32 s64, s64, s72\n\t" \
    "s_add_u32 s64, s64, s74\n\t" \
    "s_add_u32 s64, s64, s76\n\t" \
    "s_add_u32 s64, s64, s78\n\t" \
    "s_add_u32 s64, s64, s80\n\t" \
    "s_add_u32 s64, s64, s82\n\t" \
    "s_add_u32 s64, s64, s84\n\t" \
    "s_add_u32 s64, s64, s86\n\t" \
    "s_add_u32 s64, s64, s88\n\t" \
    "s_mul_i32 s62, s90, 13\n\t" \
    "s_sub_u32 s62, s62, s64\n\t" \
    "s_cmp_eq_u32 s61, 4\n\t" \
    "s_cselect_b32 s63, s62, 47\n\t" \
    "s_add_u32 s62, s62, s63\n\t" \
    "s_or_b32 s63, s61, 12\n\t" \
    "s_cmp_lg_u32 s63, 12\n\t" \
    "s_cbranch_scc1 2f\n\t" \
    "s_cmp_eq_u32 s61, 0\n\t" \
    "s_cbranch_scc1 2f\n\t" \
    "s_add_u32 %[pm_], s62, s60\n\t" \
    "s_mov_b32 %[pr_], s90\n\t" \
    "2:\n\t" \
    "s_waitcnt vmcnt(2)\n\t" \
    "s_barrier\n\t" \
    "ds_read_b128 v[128:131], %[fa] offset:0\n\t" \
    "ds_read_b128 v[132:135], %[fa] offset:1024\n\t" \
    "ds_read_b128 v[136:139], %[fa] offset:2048\n\t" \
    "ds_read_b128 v[140:143], %[fa] offset:3072\n\t" \
    ST1 \
    "s_waitcnt lgkmcnt(3)\n\t" \
    "v_smfmac_f32_16x16x64_bf16 v[168:171], v[128:131], %[v00], %[idx]\n\t" \
    FILL \
    "v_smfmac_f32_16x16x64_bf16 v[172:175], v[128:131], %[v01], %[idx]\n\t" \
    FILL \
    "s_waitcnt lgkmcnt(2)\n\t" \
    "v_smfmac_f32_16x16x64_bf16 v[168:171], v[132:135], %[v10], %[idx]\n\t" \
    FILL \
    "v_smfmac_f32_16x16x64_bf16 v[172:175], v[132:135], %[v11], %[idx]\n\t" \
    FILL \
    "s_waitcnt vmcnt(0)\n\t" \
    "s_barrier\n\t" \
    "ds_read_b128 v[144:147], %[fa] offset:4096\n\t" \
    "ds_read_b128 v[148:151], %[fa] offset:5120\n\t" \
    "ds_read_b128 v[152:155], %[fa] offset:6144\n\t" \
    "ds_read_b128 v[156:159], %[fa] offset:7168\n\t" \
    "ds_read_b128 v[160:163], %[fa] offset:8192\n\t" \
    "ds_read_b128 v[164:167], %[fa] offset:9216\n\t" \
    ST2 \
    "s_waitcnt lgkmcnt(6)\n\t" \
    "v_smfmac_f32_16x16x64_bf16 v[168:171], v[136:139], %[v20], %[idx]\n\t" \
    "buffer_store_dwordx4 v[240:243], %[ro], %[hot], %[rb0] offen sc1\n\t" \
    "v_smfmac_f32_16x16x64_bf16 v[172:175], v[136:139], %[v21], %[idx]\n\t" \
    FILL \
    "v_smfmac_f32_16x16x64_bf16 v[168:171], v[140:143], %[v30], %[idx]\n\t" \
    "buffer_store_dwordx4 v[240:243], %[ro], %[hot], %[rb1] offen sc1\n\t" \
    "v_smfmac_f32_16x16x64_bf16 v[172:175], v[140:143], %[v31], %[idx]\n\t" \
    FILL \
    "s_waitcnt lgkmcnt(5)\n\t" \
    "v_smfmac_f32_16x16x64_bf16 v[168:171], v[144:147], %[v40], %[idx]\n\t" \
    "buffer_store_dwordx2 v[240:241], %[ro2], %[hot], %[rb2] offen sc1\n\t" \
    "v_smfmac_f32_16x16x64_bf16 v[172:175], v[144:147], %[v41], %[idx]\n\t" \
    FILL \
    "s_waitcnt lgkmcnt(4)\n\t" \
    "v_smfmac_f32_16x16x64_bf16 v[168:171], v[148:151], %[v50], %[idx]\n\t" \
    "buffer_store_dwordx4 v[224:227], %[y2o], %[big], %[zb0] offen sc1\n\t" \
    "v_smfmac_f32_16x16x64_bf16 v[172:175], v[148:151], %[v51], %[idx]\n\t" \
    FILL \
    "s_waitcnt lgkmcnt(3)\n\t" \
    "v_smfmac_f32_16x16x64_bf16 v[168:171], v[152:155], %[v60], %[idx]\n\t" \
    "buffer_store_dwordx4 v[228:231], %[y2o], %[big], %[zb1] offen sc1\n\t" \
    "v_smfmac_f32_16x16x64_bf16 v[172:175], v[152:155], %[v61], %[idx]\n\t" \
    FILL \
    "s_waitcnt lgkmcnt(2)\n\t" \
    "v_smfmac_f32_16x16x64_bf16 v[168:171], v[156:159], %[v70], %[idx]\n\t" \
    "buffer_store_dwordx2 v[232:233], %[y2o2], %[big], %[zb2] offen sc1\n\t" \
    "v_smfmac_f32_16x16x64_bf16 v[172:175], v[156:159], %[v71], %[idx]\n\t" \
    FILL \
    "s_waitcnt lgkmcnt(1)\n\t" \
    "v_smfmac_f32_16x16x64_bf16 v[168:171], v[160:163], %[v80], %[idx]\n\t" \
    "s_waitcnt lgkmcnt(0)\n\t" \
    "v_smfmac_f32_16x16x64_bf16 v[168:171], v[164:167], %[v90], %[idx]\n\t" \
    FILL \
    "v_smfmac_f32_16x16x64_bf16 v[172:175], v[160:163], %[v81], %[idx]\n\t" \
    FILL1 \
    "v_smfmac_f32_16x16x64_bf16 v[172:175], v[164:167], %[v91], %[idx]\n\t" \
    ST3 \
    "s_nop 0\n\t" \
    "v_add_f32 v234, v168, v170\n\t" \
    "v_add_f32 v235, v169, v171\n\t" \
    "v_cvt_pk_bf16_f32 v238, v234, v235\n\t" \
    "ds_write_b32 %[vw], v238\n\t" \
    "v_cmp_u_f32 vcc, v234, v235\n\t" \
    "s_nop 1\n\t" \
    "v_add_f32 v236, v172, v174\n\t" \
    "v_add_f32 v237, v173, v175\n\t" \
    "v_cvt_pk_bf16_f32 v239, v236, v237\n\t" \
    "ds_write_b32 %[vw], v239 offset:256\n\t" \
    "s_andn2_b64 vcc, vcc, %[force]\n\t" \
    "s_waitcnt lgkmcnt(0)\n\t" \
    "s_barrier\n\t" \
    "ds_read_b64_tr_b16 v[176:177], %[vr] offset:0\n\t" \
    "ds_read_b64_tr_b16 v[178:179], %[vr] offset:128\n\t" \
    "ds_read_b64_tr_b16 v[180:181], %[vr] offset:1024\n\t" \
    "ds_read_b64_tr_b16 v[182:183], %[vr] offset:1152\n\t" \
    "s_cbranch_scc1 8b\n\t" \
    ST4 \
    "s_waitcnt lgkmcnt(2)\n\t" \
    "v_smfmac_f32_16x16x64_bf16 v[184:187], v[176:179], %[o00], %[idx]\n\t" \
    "s_waitcnt lgkmcnt(0)\n\t" \
    "v_smfmac_f32_16x16x64_bf16 v[184:187], v[180:183], %[o10], %[idx]\n\t" \
    FILL1 \
    "v_smfmac_f32_16x16x64_bf16 v[188:191], v[176:179], %[o01], %[idx]\n\t" \
    "s_nop 0\n\t" \
    "v_smfmac_f32_16x16x64_bf16 v[188:191], v[180:183], %[o11], %[idx]\n\t" \
    FILL1 \
    "v_smfmac_f32_16x16x64_bf16 v[192:195], v[176:179], %[o02], %[idx]\n\t" \
    "v_add_f32 v234, v184, v186\n\t" \
    "v_add_f32 v235, v185, v187\n\t" \
    "v_smfmac_f32_16x16x64_bf16 v[192:195], v[180:183], %[o12], %[idx]\n\t" \
    "v_cvt_pk_bf16_f32 v224, v234, v235\n\t" \
    "v_smfmac_f32_16x16x64_bf16 v[196:199], v[176:179], %[o03], %[idx]\n\t" \
    "v_add_f32 v234, v188, v190\n\t" \
    "v_add_f32 v235, v189, v191\n\t" \
    "v_smfmac_f32_16x16x64_bf16 v[196:199], v[180:183], %[o13], %[idx]\n\t" \
    "v_cvt_pk_bf16_f32 v225, v234, v235\n\t" \
    "v_smfmac_f32_16x16x64_bf16 v[200:203], v[176:179], %[o04], %[idx]\n\t" \
    "v_add_f32 v234, v192, v194\n\t" \
    "v_add_f32 v235, v193, v195\n\t" \
    "v_permlane16_swap_b32 v224, v225\n\t" \
    "v_smfmac_f32_16x16x64_bf16 v[200:203], v[180:183], %[o14], %[idx]\n\t" \
    "v_cvt_pk_bf16_f32 v226, v234, v235\n\t" \
    "v_smfmac_f32_16x16x64_bf16 v[204:207], v[176:179], %[o05], %[idx]\n\t" \
    "v_add_f32 v234, v196, v198\n\t" \
    "v_add_f32 v235, v197, v199\n\t" \
    "v_smfmac_f32_16x16x64_bf16 v[204:207], v[180:183], %[o15], %[idx]\n\t" \
    "v_cvt_pk_bf16_f32 v227, v234, v235\n\t" \
    "v_smfmac_f32_16x16x64_bf16 v[208:211], v[176:179], %[o06], %[idx]\n\t" \
    "v_add_f32 v234, v200, v202\n\t" \
    "v_add_f32 v235, v201, v203\n\t" \
    "v_permlane16_swap_b32 v226, v227\n\t" \
    "v_smfmac_f32_16x16x64_bf16 v[208:211], v[180:183], %[o16], %[idx]\n\t" \
    "v_cvt_pk_bf16_f32 v228, v234, v235\n\t" \
    "v_permlane32_swap_b32 v224, v226\n\t" \
    "v_smfmac_f32_16x16x64_bf16 v[212:215], v[176:179], %[o07], %[idx]\n\t" \
    "v_add_f32 v234, v204, v206\n\t" \
    "v_add_f32 v235, v205, v207\n\t" \
    "v_permlane32_swap_b32 v225, v227\n\t" \
    "buffer_store_dwordx4 v[224:227], %[ys], %[hot], %[yb0] offen sc1\n\t" \
    "v_smfmac_f32_16x16x64_bf16 v[212:215], v[180:183], %[o17], %[idx]\n\t" \
    "v_cvt_pk_bf16_f32 v229, v234, v235\n\t" \
    "v_smfmac_f32_16x16x64_bf16 v[216:219], v[176:179], %[o08], %[idx]\n\t" \
    "v_add_f32 v234, v208, v210\n\t" \
    "v_add_f32 v235, v209, v211\n\t" \
    "v_permlane16_swap_b32 v228, v229\n\t" \
    "v_smfmac_f32_16x16x64_bf16 v[216:219], v[180:183], %[o18], %[idx]\n\t" \
    "v_cvt_pk_bf16_f32 v230, v234, v235\n\t" \
    "v_smfmac_f32_16x16x64_bf16 v[220:223], v[176:179], %[o09], %[idx]\n\t" \
    "v_add_f32 v234, v212, v214\n\t" \
    "v_add_f32 v235, v213, v215\n\t" \
    "v_smfmac_f32_16x16x64_bf16 v[220:223], v[180:183], %[o19], %[idx]\n\t" \
    "v_cvt_pk_bf16_f32 v231, v234, v235\n\t" \
    ST5 \
    "v_add_f32 v234, v216, v218\n\t" \
    "v_add_f32 v235, v217, v219\n\t" \
    "v_permlane16_swap_b32 v230, v231\n\t" \
    "v_cvt_pk_bf16_f32 v232, v234, v235\n\t" \
    "s_nop 0\n\t" \
    "v_permlane32_swap_b32 v228, v230\n\t" \
    "v_permlane32_swap_b32 v229, v231\n\t" \
    "buffer_store_dwordx4 v[228:231], %[ys], %[hot], %[yb1] offen sc1\n\t" \
    "v_add_f32 v234, v220, v222\n\t" \
    "v_add_f32 v235, v221, v223\n\t" \
    "v_cvt_pk_bf16_f32 v233, v234, v235\n\t" \
    "s_nop 1\n\t" \
    "v_permlane16_swap_b32 v232, v233\n\t" \
    "buffer_store_dwordx2 v[232:233], %[ys2], %[hot], %[yb2] offen sc1\n\t" \
    ST8 \
    STR1 \
    "9:\n\t" \
    "s_waitcnt lgkmcnt(0)\n\t" \
    ""
#define B_BODY \
    "s_mov_b32 %[miss], 0\n\t" \
    "s_branch 0f\n\t" \
    "8:\n\t" \
    RST \
    "s_add_u32 %[n], %[n], 1\n\t" \
    "s_cmp_gt_u32 %[n], %[lim]\n\t" \
    "s_cselect_b32 %[miss], 1, 0\n\t" \
    "s_cbranch_scc1 7f\n\t" \
    "s_add_u32 %[slot], %[slot], %[q]\n\t" \
    "s_cmp_lg_u32 %[tm], 0\n\t" \
    "s_cselect_b32 %[tm], 2, 0\n\t" \
    "s_mov_b32 %[late], %[ex]\n\t" \
    "s_nop 7\n\t" \
    "s_waitcnt lgkmcnt(0)\n\t" \
    "0:\n\t" \
    "v_mov_b64 v[240:241], 0\n\t" \
    "v_mov_b64 v[242:243], 0\n\t" \
    "s_cmp_eq_u32 %[tm], 0\n\t" \
    "s_cbranch_scc1 5f\n\t" \
    "s_cmp_eq_u32 %[tm], 2\n\t" \
    "s_cbranch_scc1 3f\n\t" \
    "s_memtime s[60:61]\n\t" \
    "s_memrealtime s[64:65]\n\t" \
    "s_nop 0\n\t" \
    "s_memrealtime s[66:67]\n\t" \
    "s_nop 0\n\t" \
    "s_memrealtime s[68:69]\n\t" \
    "s_nop 0\n\t" \
    "s_memrealtime s[70:71]\n\t" \
    "s_nop 0\n\t" \
    "s_memrealtime s[72:73]\n\t" \
    "s_nop 0\n\t" \
    "s_memrealtime s[74:75]\n\t" \
    "s_nop 0\n\t" \
    "s_memrealtime s[76:77]\n\t" \
    "s_nop 0\n\t" \
    "s_memrealtime s[78:79]\n\t" \
    "s_nop 0\n\t" \
    "s_memrealtime s[80:81]\n\t" \
    "s_nop 0\n\t" \
    "s_memrealtime s[82:83]\n\t" \
    "s_nop 0\n\t" \
    "s_memrealtime s[84:85]\n\t" \
    "s_nop 0\n\t" \
    "s_memrealtime s[86:87]\n\t" \
    "s_nop 0\n\t" \
    "s_memrealtime s[88:89]\n\t" \
    "s_nop 0\n\t" \
    "s_memrealtime s[90:91]\n\t" \
    "s_nop 0\n\t" \
    "s_waitcnt lgkmcnt(0)\n\t" \
    "s_sub_u32 s61, s90, s64\n\t" \
    "s_add_u32 s64, s64, s66\n\t" \
    "s_add_u32 s64, s64, s68\n\t" \
    "s_add_u32 s64, s64, s70\n\t" \
    "s_add_u32 s64, s64, s72\n\t" \
    "s_add_u32 s64, s64, s74\n\t" \
    "s_add_u32 s64, s64, s76\n\t" \
    "s_add_u32 s64, s64, s78\n\t" \
    "s_add_u32 s64, s64, s80\n\t" \
    "s_add_u32 s64, s64, s82\n\t" \
    "s_add_u32 s64, s64, s84\n\t" \
    "s_add_u32 s64, s64, s86\n\t" \
    "s_add_u32 s64, s64, s88\n\t" \
    "s_mul_i32 s62, s90, 13\n\t" \
    "s_sub_u32 s62, s62, s64\n\t" \
    "s_cmp_eq_u32 s61, 4\n\t" \
    "s_cselect_b32 s63, s62, 47\n\t" \
    "s_add_u32 s62, s62, s63\n\t" \
    "s_or_b32 s63, s61, 12\n\t" \
    "s_cmp_lg_u32 s63, 12\n\t" \
    "s_cbranch_scc1 3f\n\t" \
    "s_cmp_eq_u32 s61, 0\n\t" \
    "s_cbranch_scc1 3f\n\t" \
    "s_add_u32 %[pm_], s62, s60\n\t" \
    "s_mov_b32 %[pr_], s90\n\t" \
    "3:\n\t" \
    "s_sub_u32 s60, %[slot], %[pr_]\n\t" \
    "s_add_u32 s60, s60, %[late]\n\t" \
    "s_mul_i32 s60, s60, %[rho]\n\t" \
    "s_ashr_i32 s60, s60, 8\n\t" \
    "s_add_u32 s62, s60, %[pm_]\n\t" \
    "4:\n\t" \
    "s_memtime vcc\n\t" \
    "s_waitcnt lgkmcnt(0)\n\t" \
    "s_sub_u32 vcc_lo, s62, vcc_lo\n\t" \
    "s_cmp_gt_i32 vcc_lo, 128\n\t" \
    "s_cbranch_scc1 4b\n\t" \
    "s_max_i32 vcc_lo, vcc_lo, 0\n\t" \
    "s_and_b32 vcc_lo, vcc_lo, -4\n\t" \
    "s_sub_u32 s62, 140, vcc_lo\n\t" \
    "s_getpc_b64 vcc\n\t" \
    "s_add_u32 vcc_lo, vcc_lo, s62\n\t" \
    "s_addc_u32 vcc_hi, vcc_hi, 0\n\t" \
    "s_setpc_b64 vcc\n\t" \
    ".rept 32\n\t" \
    "s_nop 0\n\t" \
    ".endr\n\t" \
    "5:\n\t" \
    "buffer_load_dwordx4 v[128:131], %[vo], %[hot], %[y0] offen offset:0 sc1\n\t" \
    STR0 \
    ST0 \
    "buffer_load_dwordx4 v[132:135], %[vo], %[hot], %[y0] offen offset:1024 sc1\n\t" \
    "buffer_load_dwordx4 v[136:139], %[vo], %[hot], %[y0] offen offset:2048 sc1\n\t" \
    "buffer_load_dwordx4 v[140:143], %[vo], %[hot], %[y0] offen offset:3072 sc1\n\t" \
    "buffer_load_dwordx4 v[144:147], %[vo], %[hot], %[y1] offen offset:0 sc1\n\t" \
    "buffer_load_dwordx4 v[148:151], %[vo], %[hot], %[y1] offen offset:1024 sc1\n\t" \
    "buffer_load_dwordx4 v[152:155], %[vo], %[hot], %[y1] offen offset:2048 sc1\n\t" \
    "buffer_load_dwordx4 v[156:159], %[vo], %[hot], %[y1] offen offset:3072 sc1\n\t" \
    "buffer_load_dwordx4 v[160:163], %[vo], %[hot], %[p4] offen offset:0 sc1\n\t" \
    "buffer_load_dwordx4 v[164:167], %[vo], %[hot], %[p4] offen offset:1024 sc1\n\t" \
    "buffer_load_dwordx4 v[168:171], %[vo], %[hot], %[p4] offen offset:2048 sc1\n\t" \
    "buffer_load_dwordx4 v[172:175], %[vo], %[hot], %[p4] offen offset:3072 sc1\n\t" \
    "buffer_load_dwordx4 v[176:179], %[vo], %[hot], %[p0] offen offset:0 sc1\n\t" \
    "buffer_load_dwordx4 v[180:183], %[vo], %[hot], %[p0] offen offset:1024 sc1\n\t" \
    "buffer_load_dwordx4 v[184:187], %[vo], %[hot], %[p0] offen offset:2048 sc1\n\t" \
    "buffer_load_dwordx4 v[188:191], %[vo], %[hot], %[p0] offen offset:3072 sc1\n\t" \
    "buffer_load_dwordx4 v[192:195], %[vo], %[hot], %[p1] offen offset:0 sc1\n\t" \
    "buffer_load_dwordx4 v[196:199], %[vo], %[hot], %[p1] offen offset:1024 sc1\n\t" \
    "buffer_load_dwordx4 v[200:203], %[vo], %[hot], %[p1] offen offset:2048 sc1\n\t" \
    "buffer_load_dwordx4 v[204:207], %[vo], %[hot], %[p1] offen offset:3072 sc1\n\t" \
    "buffer_load_dwordx4 v[208:211], %[vo], %[hot], %[p2] offen offset:0 sc1\n\t" \
    "buffer_load_dwordx4 v[212:215], %[vo], %[hot], %[p2] offen offset:1024 sc1\n\t" \
    "buffer_load_dwordx4 v[216:219], %[vo], %[hot], %[p2] offen offset:2048 sc1\n\t" \
    "buffer_load_dwordx4 v[220:223], %[vo], %[hot], %[p2] offen offset:3072 sc1\n\t" \
    "buffer_load_dwordx4 v[224:227], %[vo], %[hot], %[p3] offen offset:0 sc1\n\t" \
    "buffer_load_dwordx4 v[228:231], %[vo], %[hot], %[p3] offen offset:1024 sc1\n\t" \
    "buffer_load_dwordx4 v[232:235], %[vo], %[hot], %[p3] offen offset:2048 sc1\n\t" \
    "buffer_load_dwordx4 v[236:239], %[vo], %[hot], %[p3] offen offset:3072 sc1\n\t" \
    ST1 \
    BX \
    "s_waitcnt vmcnt(19)\n\t" \
    "v_smfmac_f32_16x16x64_bf16 v[240:243], v[160:163], v[128:135], %[idx]\n\t" \
    ST2 \
    "s_waitcnt vmcnt(18)\n\t" \
    "v_smfmac_f32_16x16x64_bf16 v[240:243], v[164:167], v[136:143], %[idx]\n\t" \
    "s_waitcnt vmcnt(17)\n\t" \
    "v_smfmac_f32_16x16x64_bf16 v[240:243], v[168:171], v[144:151], %[idx]\n\t" \
    "s_waitcnt vmcnt(16)\n\t" \
    "v_smfmac_f32_16x16x64_bf16 v[240:243], v[172:175], v[152:159], %[idx]\n\t" \
    "s_waitcnt vmcnt(15)\n\t" \
    "v_smfmac_f32_16x16x64_bf16 v[240:243], v[176:179], %[z0], %[idx]\n\t" \
    ST3 \
    "s_waitcnt vmcnt(14)\n\t" \
    "v_smfmac_f32_16x16x64_bf16 v[240:243], v[180:183], %[z1], %[idx]\n\t" \
    "s_waitcnt vmcnt(13)\n\t" \
    "v_smfmac_f32_16x16x64_bf16 v[240:243], v[184:187], %[z2], %[idx]\n\t" \
    "s_waitcnt vmcnt(12)\n\t" \
    "v_smfmac_f32_16x16x64_bf16 v[240:243], v[188:191], %[z3], %[idx]\n\t" \
    "s_waitcnt vmcnt(11)\n\t" \
    "v_smfmac_f32_16x16x64_bf16 v[240:243], v[192:195], %[z4], %[idx]\n\t" \
    "s_waitcnt vmcnt(10)\n\t" \
    "v_smfmac_f32_16x16x64_bf16 v[240:243], v[196:199], %[z5], %[idx]\n\t" \
    "s_waitcnt vmcnt(9)\n\t" \
    "v_smfmac_f32_16x16x64_bf16 v[240:243], v[200:203], %[z6], %[idx]\n\t" \
    "s_waitcnt vmcnt(8)\n\t" \
    "v_smfmac_f32_16x16x64_bf16 v[240:243], v[204:207], %[z7], %[idx]\n\t" \
    "s_waitcnt vmcnt(7)\n\t" \
    "v_smfmac_f32_16x16x64_bf16 v[240:243], v[208:211], %[z8], %[idx]\n\t" \
    "s_waitcnt vmcnt(6)\n\t" \
    "v_smfmac_f32_16x16x64_bf16 v[240:243], v[212:215], %[z9], %[idx]\n\t" \
    "s_waitcnt vmcnt(5)\n\t" \
    "v_smfmac_f32_16x16x64_bf16 v[240:243], v[216:219], %[z10], %[idx]\n\t" \
    "s_waitcnt vmcnt(4)\n\t" \
    "v_smfmac_f32_16x16x64_bf16 v[240:243], v[220:223], %[z11], %[idx]\n\t" \
    "s_waitcnt vmcnt(3)\n\t" \
    "v_smfmac_f32_16x16x64_bf16 v[240:243], v[224:227], %[z12], %[idx]\n\t" \
    "s_waitcnt vmcnt(2)\n\t" \
    "v_smfmac_f32_16x16x64_bf16 v[240:243], v[228:231], %[z13], %[idx]\n\t" \
    "s_waitcnt vmcnt(1)\n\t" \
    "v_smfmac_f32_16x16x64_bf16 v[240:243], v[232:235], %[z14], %[idx]\n\t" \
    "s_waitcnt vmcnt(0)\n\t" \
    "v_smfmac_f32_16x16x64_bf16 v[240:243], v[236:239], %[z15], %[idx]\n\t" \
    ST4 \
    "s_nop 7\n\t" \
    "v_add_f32 v245, v240, v242\n\t" \
    "v_add_f32 v246, v241, v243\n\t" \
    "v_cvt_pk_bf16_f32 v244, v245, v246\n\t" \
    "v_cmp_u_f32 vcc, v245, v246\n\t" \
    "s_nop 0\n\t" \
    "v_mov_b32_dpp v245, v244 quad_perm:[1,0,3,2] row_mask:0xf bank_mask:0xf\n\t" \
    "v_perm_b32 v244, v245, v244, %[sel]\n\t" \
    "s_cbranch_vccnz 8b\n\t" \
    "6:\n\t" \
    "buffer_store_dword v244, %[so], %[hot], %[mo] offen sc1\n\t" \
    ST8 \
    STR1 \
    "9:\n\t" \
    "s_waitcnt lgkmcnt(0)\n\t" \
    ".subsection 1\n\t" \
    "7:\n\t" \
    "s_cmp_eq_u32 %[chk], 0\n\t" \
    "s_cbranch_scc1 9b\n\t" \
    "v_max_u32 v245, v128, v129\n\t" \
    "v_max3_u32 v245, v245, v130, v131\n\t" \
    "v_max3_u32 v245, v245, v132, v133\n\t" \
    "v_max3_u32 v245, v245, v134, v135\n\t" \
    "v_max3_u32 v245, v245, v136, v137\n\t" \
    "v_max3_u32 v245, v245, v138, v139\n\t" \
    "v_max3_u32 v245, v245, v140, v141\n\t" \
    "v_max3_u32 v245, v245, v142, v143\n\t" \
    "v_max3_u32 v245, v245, v144, v145\n\t" \
    "v_max3_u32 v245, v245, v146, v147\n\t" \
    "v_max3_u32 v245, v245, v148, v149\n\t" \
    "v_max3_u32 v245, v245, v150, v151\n\t" \
    "v_max3_u32 v245, v245, v152, v153\n\t" \
    "v_max3_u32 v245, v245, v154, v155\n\t" \
    "v_max3_u32 v245, v245, v156, v157\n\t" \
    "v_max3_u32 v245, v245, v158, v159\n\t" \
    "v_max3_u32 v245, v245, v160, v161\n\t" \
    "v_max3_u32 v245, v245, v162, v163\n\t" \
    "v_max3_u32 v245, v245, v164, v165\n\t" \
    "v_max3_u32 v245, v245, v166, v167\n\t" \
    "v_max3_u32 v245, v245, v168, v169\n\t" \
    "v_max3_u32 v245, v245, v170, v171\n\t" \
    "v_max3_u32 v245, v245, v172, v173\n\t" \
    "v_max3_u32 v245, v245, v174, v175\n\t" \
    "v_max3_u32 v245, v245, v176, v177\n\t" \
    "v_max3_u32 v245, v245, v178, v179\n\t" \
    "v_max3_u32 v245, v245, v180, v181\n\t" \
    "v_max3_u32 v245, v245, v182, v183\n\t" \
    "v_max3_u32 v245, v245, v184, v185\n\t" \
    "v_max3_u32 v245, v245, v186, v187\n\t" \
    "v_max3_u32 v245, v245, v188, v189\n\t" \
    "v_max3_u32 v245, v245, v190, v191\n\t" \
    "v_max3_u32 v245, v245, v192, v193\n\t" \
    "v_max3_u32 v245, v245, v194, v195\n\t" \
    "v_max3_u32 v245, v245, v196, v197\n\t" \
    "v_max3_u32 v245, v245, v198, v199\n\t" \
    "v_max3_u32 v245, v245, v200, v201\n\t" \
    "v_max3_u32 v245, v245, v202, v203\n\t" \
    "v_max3_u32 v245, v245, v204, v205\n\t" \
    "v_max3_u32 v245, v245, v206, v207\n\t" \
    "v_max3_u32 v245, v245, v208, v209\n\t" \
    "v_max3_u32 v245, v245, v210, v211\n\t" \
    "v_max3_u32 v245, v245, v212, v213\n\t" \
    "v_max3_u32 v245, v245, v214, v215\n\t" \
    "v_max3_u32 v245, v245, v216, v217\n\t" \
    "v_max3_u32 v245, v245, v218, v219\n\t" \
    "v_max3_u32 v245, v245, v220, v221\n\t" \
    "v_max3_u32 v245, v245, v222, v223\n\t" \
    "v_max3_u32 v245, v245, v224, v225\n\t" \
    "v_max3_u32 v245, v245, v226, v227\n\t" \
    "v_max3_u32 v245, v245, v228, v229\n\t" \
    "v_max3_u32 v245, v245, v230, v231\n\t" \
    "v_max3_u32 v245, v245, v232, v233\n\t" \
    "v_max3_u32 v245, v245, v234, v235\n\t" \
    "v_max3_u32 v245, v245, v236, v237\n\t" \
    "v_max3_u32 v245, v245, v238, v239\n\t" \
    "v_cmp_eq_u32 vcc, -1, v245\n\t" \
    "s_nop 4\n\t" \
    "s_cbranch_vccnz 9b\n\t" \
    "s_mov_b32 %[miss], 0\n\t" \
    "s_branch 6b\n\t" \
    ".fill 64, 4, 0xBF800000\n\t" \
    ".subsection 0\n\t" \
    ""
#define A_W [w00] "a"(W[0][0]), [w01] "a"(W[0][1]), [w02] "a"(W[0][2]), [w03] "a"(W[0][3]), [w10] "a"(W[1][0]), [w11] "a"(W[1][1]), [w12] "a"(W[1][2]), [w13] "a"(W[1][3]), [w20] "a"(W[2][0]), [w21] "a"(W[2][1]), [w22] "a"(W[2][2]), [w23] "a"(W[2][3]), [w30] "a"(W[3][0]), [w31] "a"(W[3][1]), [w32] "a"(W[3][2]), [w33] "a"(W[3][3]), [w40] "a"(W[4][0]), [w41] "a"(W[4][1]), [w42] "a"(W[4][2]), [w43] "a"(W[4][3]), [w50] "a"(W[5][0]), [w51] "a"(W[5][1]), [w52] "a"(W[5][2]), [w53] "a"(W[5][3]), [w60] "a"(W[6][0]), [w61] "a"(W[6][1]), [w62] "a"(W[6][2]), [w63] "a"(W[6][3]), [w70] "a"(W[7][0]), [w71] "a"(W[7][1]), [w72] "a"(W[7][2]), [w73] "a"(W[7][3]), [w80] "v"(W[8][0]), [w81] "v"(W[8][1]), [w82] "v"(W[8][2]), [w83] "v"(W[8][3]), [w90] "v"(W[9][0]), [w91] "v"(W[9][1]), [w92] "v"(W[9][2]), [w93] "v"(W[9][3])
#define C_W [v00] "a"(Wv[0][0]), [v01] "a"(Wv[0][1]), [v10] "a"(Wv[1][0]), [v11] "a"(Wv[1][1]), [v20] "a"(Wv[2][0]), [v21] "a"(Wv[2][1]), [v30] "a"(Wv[3][0]), [v31] "a"(Wv[3][1]), [v40] "a"(Wv[4][0]), [v41] "a"(Wv[4][1]), [v50] "a"(Wv[5][0]), [v51] "a"(Wv[5][1]), [v60] "a"(Wv[6][0]), [v61] "a"(Wv[6][1]), [v70] "a"(Wv[7][0]), [v71] "a"(Wv[7][1]), [v80] "a"(Wv[8][0]), [v81] "a"(Wv[8][1]), [v90] "a"(Wv[9][0]), [v91] "a"(Wv[9][1]), [o00] "a"(Wo[0][0]), [o01] "a"(Wo[0][1]), [o02] "a"(Wo[0][2]), [o03] "a"(Wo[0][3]), [o04] "a"(Wo[0][4]), [o05] "a"(Wo[0][5]), [o06] "a"(Wo[0][6]), [o07] "a"(Wo[0][7]), [o08] "a"(Wo[0][8]), [o09] "a"(Wo[0][9]), [o10] "a"(Wo[1][0]), [o11] "a"(Wo[1][1]), [o12] "v"(Wo[1][2]), [o13] "v"(Wo[1][3]), [o14] "v"(Wo[1][4]), [o15] "v"(Wo[1][5]), [o16] "v"(Wo[1][6]), [o17] "v"(Wo[1][7]), [o18] "v"(Wo[1][8]), [o19] "v"(Wo[1][9])
#define B_Z [z0] "a"(z[0]), [z1] "a"(z[1]), [z2] "a"(z[2]), [z3] "a"(z[3]), [z4] "a"(z[4]), [z5] "a"(z[5]), [z6] "a"(z[6]), [z7] "a"(z[7]), [z8] "a"(z[8]), [z9] "a"(z[9]), [z10] "a"(z[10]), [z11] "a"(z[11]), [z12] "a"(z[12]), [z13] "a"(z[13]), [z14] "a"(z[14]), [z15] "a"(z[15])
#define CLOB "v128", "v129", "v130", "v131", "v132", "v133", "v134", "v135", "v136", "v137", "v138", "v139", "v140", "v141", "v142", "v143", "v144", "v145", "v146", "v147", "v148", "v149", "v150", "v151", "v152", "v153", "v154", "v155", "v156", "v157", "v158", "v159", "v160", "v161", "v162", "v163", "v164", "v165", "v166", "v167", "v168", "v169", "v170", "v171", "v172", "v173", "v174", "v175", "v176", "v177", "v178", "v179", "v180", "v181", "v182", "v183", "v184", "v185", "v186", "v187", "v188", "v189", "v190", "v191", "v192", "v193", "v194", "v195", "v196", "v197", "v198", "v199", "v200", "v201", "v202", "v203", "v204", "v205", "v206", "v207", "v208", "v209", "v210", "v211", "v212", "v213", "v214", "v215", "v216", "v217", "v218", "v219", "v220", "v221", "v222", "v223", "v224", "v225", "v226", "v227", "v228", "v229", "v230", "v231", "v232", "v233", "v234", "v235", "v236", "v237", "v238", "v239", "v240", "v241", "v242", "v243", "v244", "v245", "v246", "v247", "v248", "v249", "v250", "v251", "v252", "v253", "v254", "v255"
#define CLOBC "v128", "v129", "v130", "v131", "v132", "v133", "v134", "v135", "v136", "v137", "v138", "v139", "v140", "v141", "v142", "v143", "v144", "v145", "v146", "v147", "v148", "v149", "v150", "v151", "v152", "v153", "v154", "v155", "v156", "v157", "v158", "v159", "v160", "v161", "v162", "v163", "v164", "v165", "v166", "v167", "v168", "v169", "v170", "v171", "v172", "v173", "v174", "v175", "v176", "v177", "v178", "v179", "v180", "v181", "v182", "v183", "v184", "v185", "v186", "v187", "v188", "v189", "v190", "v191", "v192", "v193", "v194", "v195", "v196", "v197", "v198", "v199", "v200", "v201", "v202", "v203", "v204", "v205", "v206", "v207", "v208", "v209", "v210", "v211", "v212", "v213", "v214", "v215", "v216", "v217", "v218", "v219", "v220", "v221", "v222", "v223", "v234", "v235", "v236", "v237", "v238", "v239", "v240", "v241", "v242", "v243", "v244", "v245", "v246", "v247", "v248", "v249", "v250", "v251", "v252", "v253", "v254", "v255"
#define CLOBS "s60", "s61", "s62", "s63", "s64", "s65", "s66", "s67", "s68", "s69", "s70", "s71", "s72", "s73", "s74", "s75", "s76", "s77", "s78", "s79", "s80", "s81", "s82", "s83", "s84", "s85", "s86", "s87", "s88", "s89", "s90", "s91"

template <int RING, class ISSUE, class SINK, class HOOK> DI void load_weights(const Ctx& c, ISSUE issue, SINK sink, HOOK hook) {
    constexpr int NB = 80;
    const u32 ring = c.wave * (RING * 1024);
    const u32 gb = c.lane >> 4;
    const u32 t0 = ring + (c.lane >> 2) * 64 + ((gb & 1) * 32) + (c.lane & 3) * 8, t1 = t0 ^ 32;
    sfor<RING>([&](auto B) { issue(B, ring + CI(B) * 1024); });
    sfor<NB / 4>([&](auto C) {
        constexpr int ch = CI(C), issued = (RING + 4 * ch < NB) ? RING + 4 * ch : NB;
        wait_vm<issued - 4 * ch - 4>();
        hook(C);
        sfor<4>([&](auto U) {
            constexpr int b = 4 * ch + CI(U);
            v2u lo = trd(t0 + (b % RING) * 1024), hi = trd(t1 + (b % RING) * 1024);
            sink(std::integral_constant<int, b>{}, lo, hi);
        });
        if constexpr (4 * ch + RING < NB) {
            wait_lgkm();
            sfor<4>([&](auto U) { constexpr int b = 4 * ch + RING + CI(U); issue(std::integral_constant<int, b>{}, ring + (b % RING) * 1024); });
        }
    });
    wait_lgkm();
}
DI u32 dma_chunk(u32 lane) { return ((lane & 3) ^ (((lane >> 4) & 1) << 1)) * 16; }
DI u32 row_f1(u32 lane) { const u32 gb = lane >> 4, r = (lane >> 2) & 3; return 16 * (gb >> 1) + 4 * (gb & 1) + (r & 1) + 8 * (r >> 1); }
DI u32 row_f2(u32 lane) { return 8 * (lane >> 4) + ((lane >> 2) & 3); }
DI u32 row_f3(u32 lane) { return lane >> 2; }
DI v8u cat8(v2u a, v2u b, v2u c, v2u d) { return v8u{a[0], a[1], b[0], b[1], c[0], c[1], d[0], d[1]}; }
DI v4u cat4(v2u a, v2u b) { return v4u{a[0], a[1], b[0], b[1]}; }

DI u32 spin_s(u32 x) { asm volatile("" : "+s"(x)); return x; }
template <bool AG> DI void smf1(f32x4& acc, const v4u& f, const v8u& w, u32 idx) {
    if constexpr (AG) asm volatile("v_smfmac_f32_16x16x64_bf16 %0, %1, %2, %3" : "+v"(acc) : "v"(f), "a"(w), "v"(idx) : "memory");
    else asm volatile("v_smfmac_f32_16x16x64_bf16 %0, %1, %2, %3" : "+v"(acc) : "v"(f), "v"(w), "v"(idx) : "memory");
}
DI void mf(f32x4& acc, const v4u& a, const v4u& b) { asm volatile("v_mfma_f32_16x16x32_bf16 %0, %1, %2, %0" : "+v"(acc) : "v"(a), "v"(b) : "memory"); }
DI void mf0(f32x4& acc, const v4u& a, const v4u& b) { asm volatile("v_mfma_f32_16x16x32_bf16 %0, %1, %2, 0" : "=&v"(acc) : "v"(a), "v"(b) : "memory"); }
DI float vadd(float a, float b) { float r; asm volatile("v_add_f32 %0, %1, %2" : "=v"(r) : "v"(a), "v"(b) : "memory"); return r; }
DI u32 vcvt(float a, float b) { u32 r; asm volatile("v_cvt_pk_bf16_f32 %0, %1, %2" : "=v"(r) : "v"(a), "v"(b) : "memory"); return r; }
DI u64 vnan(float a, float b) { u64 m; asm volatile("v_cmp_u_f32 %0, %1, %2" : "=s"(m) : "v"(a), "v"(b) : "memory"); return m; }
DI void sw16(u32& a, u32& b) { asm volatile("v_permlane16_swap_b32 %0, %1" : "+v"(a), "+v"(b)::"memory"); }
DI void sw32(u32& a, u32& b) { asm volatile("v_permlane32_swap_b32 %0, %1" : "+v"(a), "+v"(b)::"memory"); }
template <int N> DI void nop() { asm volatile("s_nop %0" ::"i"(N) : "memory"); }
DI void pin(f32x4& x) { asm volatile("" : "+v"(x)); }
DI void pin(v4u& x) { asm volatile("" : "+v"(x)); }
DI void pina(v8u& x) { asm volatile("" : "+a"(x)); }
DI void zero(f32x4& x) { x = f32x4{}; pin(x); }
DI void nop2() { asm volatile("s_nop 1"); }
DI void settle(f32x4& x) { asm volatile("s_nop 7" : "+v"(x)); }
DI u32 vmax4(u32 mm, const v4u& f) { return umax3(umax3(mm, f[0], f[1]), f[2], f[3]); }
DI bool any_sent(u32 mm) { return __builtin_amdgcn_ballot_w64(mm == SENT) != 0; }
DI void bar0() { asm volatile("s_barrier" ::: "memory"); }
#define PAUSE 0
struct MemFetch { u32 l0, l1, l2, v0, v2; };
DI void fetch_mem(const MemFetch& m, rsrc_t r, u32 s0, u32 s1, u32 s2) {
    asm volatile(
        "s_mov_b32 m0, %0\n\ts_nop 0\n\tbuffer_load_dwordx4 %3, %5, %6 offen sc1 lds\n\t"
        "s_mov_b32 m0, %1\n\ts_nop %9\n\tbuffer_load_dwordx4 %3, %5, %7 offen sc1 lds\n\t"
        "s_mov_b32 m0, %2\n\ts_nop 0\n\tbuffer_load_dwordx4 %4, %5, %8 offen sc1 lds"
        ::"s"(m.l0), "s"(m.l1), "s"(m.l2), "v"(m.v0), "v"(m.v2), "s"(r), "s"(s0), "s"(s1), "s"(s2), "i"(PAUSE) : "memory", "m0");
}
DI MemFetch mk_fetch(const Ctx& c, u32 lds) {
    MemFetch m;
    m.l0 = spin_s(lds + c.wave * 1024); m.l1 = spin_s(lds + 4096 + c.wave * 1024); m.l2 = spin_s(lds + 8192 + c.wave * 1024);
    m.v0 = c.lane * 16 + c.wave * 1024; m.v2 = m.v0 | (c.wave >= 2 ? OOB : 0u);
    return m;
}
DI bool image_incomplete(u32 la) {
    u32 mm = 0;
    sfor<10>([&](auto K) { mm = vmax4(mm, lr16(la + CI(K) * 1024)); });
    return any_sent(mm);
}
DI void wait_flags4(Ctx& c, u32 word0, u32 need) {
    u32 n = 0;
    for (;;) {
        fence_c();
        v4u f = ld16(c.flg, word0 * 4 + (c.lane & 31) * 16, 0);
        u32 mn = min(min(f[0], f[1]), min(f[2], f[3]));
        if (__builtin_amdgcn_ballot_w64(mn < need) == 0) break;
        if (spin(c, n)) break;
        __builtin_amdgcn_s_sleep(2);
    }
}

DI void gate(Ctx& c, u32 vo, u32 so) {
    u32 n = 0;
    for (;;) {
        fence_c();
        if (__builtin_amdgcn_ballot_w64(ld4(c.hot, vo, so) == SENT) == 0 || spin(c, n)) break;
    }
}
DI u32 gate_mem(u32 lane) { return lane < 40 ? (lane >> 5) * 16384 + (lane & 31) * 256 : OOB; }
DI void greet(Ctx& c, u32 old) { c.word = (old | 3u) + 1; flag(c, c.word, F_CH + c.id); }
DI void greet(Ctx& c) { if (c.wave == 0) greet(c, rfl(ld4(c.flg, (F_RS + c.id) * 4, 0))); }
constexpr u32 L_GREET = 112 * 1024, L_GREET2 = 144 * 1024;
DI void greeted(Ctx& c, u32 l, u32 seen) {
    if (c.wave) return;
    u32 n = 0, d = seen ^ c.word;
    while (d > 3u && !spin(c, n)) {
        fence_c();
        d = rfl(ld4(c.flg, (F_RS + c.id) * 4, 0)) ^ c.word;
        if (d > 3u) __builtin_amdgcn_s_sleep(2);
    }
    lw4(l, d);
}
DI void greeted(Ctx& c, u32 l) { greeted(c, l, c.word - 1); }
DI void greeted_late(Ctx& c, u32 l) { greeted(c, l, rfl(*(volatile LAS u32*)l)); }
DI void done(Ctx& c) { if (c.wave == 0) flag(c, 1u, F_DONE + c.id); }
DI void get_par(Ctx& c, u32 l) { const u32 d = rfl(*(volatile LAS u32*)l); c.par0 = d & 1; c.par1 = (d >> 1) & 1; }
DI void greet_ask(Ctx& c, u32 l = L_GREET) {
    if (c.wave == 0) asm volatile("s_mov_b32 m0, %0\n\ts_nop 0\n\tbuffer_load_dword %1, %2, 0 offen sc1 lds" ::"s"(l), "v"((F_RS + c.id) * 4), "s"(c.flg) : "memory", "m0");
}
DI void touch(const Ctx& c, u32 l, u32 vo, rsrc_t r) {
    asm volatile("s_mov_b32 m0, %0\n\ts_nop 0\n\tbuffer_load_dword %1, %2, 0 offen sc1 lds" ::"s"(l + 256 + c.wave * 256), "v"(vo), "s"(r) : "memory", "m0");
}
DI void greet_tell(Ctx& c, u32 l = L_GREET) { if (c.wave == 0) greet(c, rfl(*(volatile LAS u32*)l)); }
#define ASK2 15
template <int CH> DI void greet_on(Ctx& c, u32 l) {
    if constexpr (CH == 0) greet_tell(c, l);
    if constexpr (CH == ASK2) greet_ask(c, l);
}
struct Args {
    const u16 *x, *mem0, *wq, *wk, *wv, *wo;
    u16* out;
    char* ws;
};

constexpr float SCALE = 0.08838834764831845f, LOG2E = 1.4426950408889634f;

DI u32 sp_idx(u32 lane) { return (lane & 2) ? 0xEEEEu : 0x4444u; }
DI u32 rho(u32 t, u32 half) { return 4 * (t >> 1) + 2 * half + (t & 1); }
#define DA 0
#define DB 0
constexpr float SC2 = SCALE * LOG2E;

#define LIMA 22
#define LIMB 10
#define BEX 8
#define BEX2 0
#define BGA SC1
#define QUIET 4
#define QUIETv QUIET
#define GAP 150
#define XSLEEP 2
constexpr u32 LA_M = 49152, LA_Q = 61440, LA_K = 63488, LA_P = 69632, LA_J = 81920;
template <int WK> DI void loop_A(Ctx& c, u32 h, const v8u (&W)[10][4]) {
    const u32 lane = c.lane, w = c.wave;
    u32 known = 0, next = 0;
    auto seen = [&](const v4u& f0, const v4u& f1) {
        const u32 m0 = rfl(min(min(f0[0], f0[1]), min(f0[2], f0[3]))), m1 = rfl(min(min(f1[0], f1[1]), min(f1[2], f1[3])));
        known = m0 <= m1 ? 2 * m0 : 2 * m1 + 1;
    };
    auto poll = [&]() { fence_c(); seen(ld16(c.flg, (F_QK + h * 4) * 4, 0), ld16(c.flg, (F_QK + (32 + h) * 4) * 4, 0)); };
    const u32 pvo = lane < 2 ? (F_QK + (32 * lane + h) * 4) * 4 : OOB;
    auto ask = [&]() { dma16c(LA_P, pvo, c.flg, 0); };
    auto fetch = [&]() {
        const u32 dst = (next % 3) * 16384, src = hf(c, h, next);
        sfor<16>([&](auto I) { dma16c(dst + CI(I) * 1024, lane * 16, c.big, src + CI(I) * 1024); });
        ++next;
    };
    auto prefetch = [&](u32 s) {
        u32 n = 0;
        while (next <= s && s < NSEG) {
            if (known <= next) poll();
            if (known > next) { fetch(); continue; }
            if (spin(c, n)) break;
            __builtin_amdgcn_s_sleep(2);
        }
        ask();
    };
    if constexpr (WK == 2) prefetch(0);
    EV(3);
    const MemFetch mfe = mk_fetch(c, LA_M);
    const u32 idx = sp_idx(lane);
    const u32 g = lane >> 4, part = (lane >> 3) & 1, r4 = (lane >> 2) & 3, c4 = lane & 3, tq = lane & 7;
    const u32 fa = LA_M + lane * 16;
    const u32 qw = LA_Q + (w * 32 + (lane & 15)) * 16 + g * 4, kw = LA_K + (w * 32 + (lane & 15)) * 32 + g * 8;
    const u32 qr = LA_Q + (4 * g + r4) * 16 + (c4 & 1) * 8, kr = LA_K + (4 * g + r4) * 32 + c4 * 8;
    const u32 hu = spin_s((h >> 4) * 16384 + (h & 15) * 512), hm = spin_s(32768 + (h >> 3) * 1024 + ((h >> 1) & 3) * 256 + (h & 1) * 8);
    const u32 puo = (g >> 1) * 256 + rho(tq, part) * 16 + (g & 1) * 8;
    const u32 pmo = (rho(tq, g & 1) * 16 + (g >> 1) * 4) | (part ? OOB : 0u);
    u32 sg = 0;
    Clk k;
    if constexpr (WK == 0) clk_init(c, k, 0);
    const u64 pm = 0xff00ff00ff00ff00ull;
    TSDECL;
    for (u32 s = 0; s < NSEG; ++s) {
        const u32 kq = (s % 3) * 16384 + lane * 16;
        const u32 mb0 = mem_slot(c, sg), mb1 = mb0 + 4096, mb2 = mb0 + 16384;
        const u32 pb = p_slot(c, sg) + hu, pb2 = p_slot(c, sg) + hm;
        const u32 last = s + 1 < NSEG ? 0u : OOB;
        const u32 rb = p_slot(c, sg == 2 ? 0 : sg + 1) + hu, rb2 = rb - hu + hm, no2 = s < 2 ? OOB : 0u;
        const bool more = next <= s + 2 && next < NSEG && known > next;
        const u32 ds = rfl(hf(c, h, next));
        u32 dl = rfl(more ? (next % 3) * 16384 : LA_J), dv = more ? lane * 16 : OOB, rpu = puo | no2, rpm = pmo | no2;
        u32 n = 0, m = 0;
        u64 force = 0;
        for (;;) {
            u32 miss;
            const u32 lim = rfl(s > 1 ? n + 3 : n);
            if constexpr (WK == 0) {
                if (s == 1 && m == 0) gate(c, gate_mem(lane), mb0);
                u32 tm = clk_mode(c, k, s && !force);
                TS(6); TS7;
                asm volatile(A_BODY0
                    : TRY_OUT, [rpu] "+v"(rpu), [rpm] "+v"(rpm), CLK_OUT STOPS
                    : A_W, [idx] "v"(idx), [fa] "v"(fa), [kq] "v"(kq), [qw] "v"(qw), [kw] "v"(kw), [qr] "v"(qr), [kr] "v"(kr), [l0] "s"(mfe.l0), [l1] "s"(mfe.l1), [l2] "s"(mfe.l2),
                      [v0] "v"(mfe.v0), [v2] "v"(mfe.v2), [hot] "s"(c.hot), [mb0] "s"(mb0), [mb1] "s"(mb1), [mb2] "s"(mb2), [force] "s"(force),
                      [puo] "v"(puo | last), [pmo] "v"(pmo | last), [pb] "s"(pb), [pb2] "s"(pb2), [sc] "s"(SC2), [pm] "s"(pm),
                      [rb] "s"(rb), [rb2] "s"(rb2), TRY_IN, CLK_IN
                    : "memory", "vcc", "scc", "m0", CLOB, CLOBS);
            } else if constexpr (WK == 2)
                asm volatile(A_BODY3
                    : TRY_OUT, [dv] "+v"(dv), [dl] "+s"(dl) STOPS
                    : A_W, [idx] "v"(idx), [fa] "v"(fa), [qw] "v"(qw), [kw] "v"(kw), [l0] "s"(mfe.l0), [l1] "s"(mfe.l1), [l2] "s"(mfe.l2),
                      [v0] "v"(mfe.v0), [v2] "v"(mfe.v2), [hot] "s"(c.hot), [mb0] "s"(mb0), [mb1] "s"(mb1), [mb2] "s"(mb2), [force] "s"(force),
                      [junk] "i"(LA_J), [ds] "s"(ds), [big] "s"(c.big), TRY_IN
                    : "memory", "vcc", "scc", "m0", "s60", CLOB);
            else
                asm volatile(A_BODY1
                    : TRY_OUT STOPS
                    : A_W, [idx] "v"(idx), [fa] "v"(fa), [qw] "v"(qw), [kw] "v"(kw), [l0] "s"(mfe.l0), [l1] "s"(mfe.l1), [l2] "s"(mfe.l2),
                      [v0] "v"(mfe.v0), [v2] "v"(mfe.v2), [hot] "s"(c.hot), [mb0] "s"(mb0), [mb1] "s"(mb1), [mb2] "s"(mb2), [force] "s"(force), TRY_IN
                    : "memory", "vcc", "scc", "m0", CLOB);
            if (__builtin_expect(miss == 0, 1)) break;
            if ((m & 3) == 3 && !image_incomplete(fa)) force = ~0ull;
            if (spin(c, m)) force = ~0ull;
            dv = rpu = rpm = OOB; dl = LA_J;
        }
        if constexpr (WK == 0) {
            TS(4); TS(5);
            clk_next(c, k, TPv(s));
            TSAVE(s, n);
        } else {
            if constexpr (WK == 1) {
                if (s + 1 < NSEG) __builtin_amdgcn_s_sleep(XSLEEP);
                fence_c();
                v4u kx[2][4], qf[4], km[4];
                f32x4 t0, t1, t2;
                sfor<8>([&](auto I) { kx[CI(I) >> 2][CI(I) & 3] = lr16(kq + CI(I) * 1024); });
                sfor<4>([&](auto E) { qf[CI(E)] = lr16(kq + 8192 + (w - 1) * 4096 + CI(E) * 1024); });
                sfor<4>([&](auto E) { km[CI(E)] = cat4(trd(kr + CI(E) * 1024), trd(kr + CI(E) * 1024 + 512)); });
                mf0(t1, kx[0][0], qf[0]); mf0(t2, kx[1][0], qf[0]); mf0(t0, km[0], qf[0]);
                sfor<3>([&](auto E) { mf(t1, kx[0][CI(E) + 1], qf[CI(E) + 1]); mf(t2, kx[1][CI(E) + 1], qf[CI(E) + 1]); mf(t0, km[CI(E) + 1], qf[CI(E) + 1]); });
                settle(t0); pin(t1); pin(t2);
                const u32 q = (w - 1) * 16 + (lane & 15);
                float v[10];
                sfor<4>([&](auto R) {
                    v[CI(R)] = (4 * g + CI(R) <= q) ? t1[CI(R)] * SCALE : -1e9f;
                    v[4 + CI(R)] = (16 + 4 * g + CI(R) <= q) ? t2[CI(R)] * SCALE : -1e9f;
                });
                v[8] = t0[0] * SCALE; v[9] = t0[1] * SCALE;
                float mx = v[9];
                sfor<9>([&](auto R) { mx = fmaxf(mx, v[CI(R)]); });
                mx = max16(mx); mx = max32(mx);
                const float mb = mx * LOG2E;
                float sm = 0.f;
                sfor<10>([&](auto R) { v[CI(R)] = __builtin_amdgcn_exp2f(v[CI(R)] * LOG2E - mb); sm += v[CI(R)]; });
                sm = sum16(sm); sm = sum32(sm);
                const float inv = __builtin_amdgcn_rcpf(sm);
                sfor<10>([&](auto R) { v[CI(R)] *= inv; });
                st16(v4u{pk(v[0], v[1]), pk(v[4], v[5]), pk(v[2], v[3]), pk(v[6], v[7])}, c.big, lane * 16, spin_s(hn(c, h, s) + H_PU + (w - 1) * 1024));
                st4(pk(v[8], v[9]), c.big, (lane & 15) * 16 + g * 4, spin_s(hn(c, h, s) + H_PM + (w - 1) * 256));
            } else {
                next += more;
                seen(lr16(LA_P), lr16(LA_P + 16));
                flag(c, s, F_A + 32 * h);
                prefetch(s + 1);
            }
        }
        sg = sg == 2 ? 0 : sg + 1;
    }
    wait_vm<0>();
    bar();
    if constexpr (WK == 2) flag(c, NSEG, F_A + 32 * h);
    if constexpr (WK == 0) { DUMP(h); }
}
DI void role_A(Ctx& c, const Args& a, u32 h) {
    const u32 lane = c.lane, w = c.wave;
    v8u W[10][4];
    greet_ask(c, L_GREET2);
    if (w == 3) touch(c, L_GREET2, lane ? OOB : hf(c, h, 0), c.big); else touch(c, L_GREET2, lane < 2 ? lane << 21 : OOB, c.hot);
    {
        v2u t[10][4][4];
        const rsrc_t rq = mkrs(a.wq, 640u * 8192), rk = mkrs(a.wk, 640u * 8192);
        const u32 vo = row_f1(lane) * 8192 + (h * 128 + w * 32) * 2 + dma_chunk(lane);
        load_weights<36>(c,
            [&](auto B, u32 dst) { constexpr int b = CI(B), m = b / 40, ks = (b % 40) / 4, tt = b % 4; dma16(dst, vo, m ? rk : rq, (64 * ks + 32 * (tt >> 1) + 2 * (tt & 1)) * 8192u); },
            [&](auto B, v2u lo, v2u hi) {
                constexpr int b = CI(B), m = b / 40, ks = (b % 40) / 4, tt = b % 4;
                t[ks][2 * m][tt] = lo; t[ks][2 * m + 1][tt] = hi;
                if constexpr (tt == 3) {
                    W[ks][2 * m] = cat8(t[ks][2 * m][0], t[ks][2 * m][1], t[ks][2 * m][2], t[ks][2 * m][3]);
                    W[ks][2 * m + 1] = cat8(t[ks][2 * m + 1][0], t[ks][2 * m + 1][1], t[ks][2 * m + 1][2], t[ks][2 * m + 1][3]);
                    if constexpr (ks < 8) { asm volatile("" : "+a"(W[ks][2 * m])); asm volatile("" : "+a"(W[ks][2 * m + 1])); }
                    else { asm volatile("" : "+v"(W[ks][2 * m])); asm volatile("" : "+v"(W[ks][2 * m + 1])); }
                }
            }, [&](auto CH) { greet_on<CI(CH)>(c, L_GREET2); });
    }
    greeted_late(c, L_GREET2);
    bar();
    get_par(c, L_GREET2);
    done(c);
    EV(2);
    if (w == 0) loop_A<0>(c, h, W); else if (w == 3) loop_A<2>(c, h, W); else loop_A<1>(c, h, W);
}

DI void role_C(Ctx& c, const Args& a, u32 h) {
    const u32 lane = c.lane, w = c.wave;
    v8u Wv[10][2], Wo[2][10];
    greet_ask(c, L_GREET2);
    touch(c, L_GREET2, lane < 2 ? lane << 21 : OOB, c.hot);
    {
        v2u t[2][4];
        const rsrc_t rv = mkrs(a.wv, 640u * 8192), ro = mkrs(a.wo, 4096u * 1280);
        const u32 vo = row_f1(lane) * 8192 + (h * 128 + w * 32) * 2 + dma_chunk(lane);
        const u32 vo2 = (h * 128 + row_f1(lane)) * 1280 + w * 320 + dma_chunk(lane);
        load_weights<36>(c,
            [&](auto B, u32 dst) {
                constexpr int b = CI(B), tt = b % 4;
                if constexpr (b < 40) dma16(dst, vo, rv, (64 * (b / 4) + 32 * (tt >> 1) + 2 * (tt & 1)) * 8192u);
                else { constexpr int u = (b - 40) / 4, ks = u / 5, cp = u % 5; dma16(dst, vo2, ro, (64 * ks + 32 * (tt >> 1) + 2 * (tt & 1)) * 1280u + cp * 64); }
            },
            [&](auto B, v2u lo, v2u hi) {
                constexpr int b = CI(B), tt = b % 4;
                t[0][tt] = lo; t[1][tt] = hi;
                if constexpr (tt == 3) {
                    v8u w0 = cat8(t[0][0], t[0][1], t[0][2], t[0][3]), w1 = cat8(t[1][0], t[1][1], t[1][2], t[1][3]);
                    if constexpr (b < 64) { asm volatile("" : "+a"(w0)); asm volatile("" : "+a"(w1)); }
                    else { asm volatile("" : "+v"(w0)); asm volatile("" : "+v"(w1)); }
                    if constexpr (b < 40) { Wv[b / 4][0] = w0; Wv[b / 4][1] = w1; }
                    else { constexpr int u = (b - 40) / 4, ks = u / 5, cp = u % 5; Wo[ks][2 * cp] = w0; Wo[ks][2 * cp + 1] = w1; }
                }
            }, [&](auto CH) { greet_on<CI(CH)>(c, L_GREET2); });
    }
    greeted_late(c, L_GREET2);
    bar();
    get_par(c, L_GREET2);
    done(c);
    EV(2);
    constexpr u32 LC_M = 0, LC_V = 12288;
    const MemFetch mfe = mk_fetch(c, LC_M);
    const u32 idx = sp_idx(lane);
    const u32 g = lane >> 4, r4 = (lane >> 2) & 3, c4 = lane & 3, c16 = lane & 15;
    const u32 fa = LC_M + lane * 16;
    const u32 vw = LC_V + (2 * w * 8 + (c16 & 7)) * 32 + g * 8 + (c16 >> 3) * 4;
    const u32 vr = LC_V + (g * 8 + r4) * 32 + c4 * 8;
    const u32 yo = g * 16384 + h * 256 + c16 * 16, yo2 = (g & 1) * 16384 + h * 256 + c16 * 16 + (g >> 1) * 8;
    const u32 y2o = g * 256 + c16 * 16, y2o2 = (g & 1) * 256 + c16 * 16 + (g >> 1) * 8;
    u32 sg = 0;
    Clk k;
    clk_init(c, k, 0);
    v4u ya = {}, yb = {};
    v2u yc = {};
    TSDECL;
    for (u32 s = 0; s < NSEG; ++s) {
        const u32 mb0 = mem_slot(c, sg), mb1 = mb0 + 4096, mb2 = mb0 + 16384;
        const u32 yb0 = y_slot(c, sg) + 10 * w * 16384, yb1 = yb0 + 4 * 16384, yb2 = yb0 + 8 * 16384;
        const u32 zb0 = hn(c, h, s - 1) + y2(10 * w), zb1 = zb0 + 1024, zb2 = zb0 + 2048;
        const u32 rb0 = y_slot(c, sg == 2 ? 0 : sg + 1) + 10 * w * 16384, rb1 = rb0 + 4 * 16384, rb2 = rb0 + 8 * 16384;
        const u32 last = s + 1 < NSEG ? 0u : OOB, no1 = s < 1 ? OOB : 0u, no2 = s < 2 ? OOB : 0u;
        u32 n = 0, m = 0, za = y2o | no1, zb = y2o2 | no1, ra = yo | no2, rb = yo2 | no2;
        u64 force = 0;
        for (;;) {
            u32 miss;
            const u32 lim = rfl(s > 1 ? n + 3 : n);
            if (w == 0 && s == 1 && m == 0) gate(c, gate_mem(lane), mb0);
            u32 tm = clk_mode(c, k, w == 0 && s && !force);
            TS(6); TS7;
            asm volatile(C_BODY
                : TRY_OUT, [y2o] "+v"(za), [y2o2] "+v"(zb), [ro] "+v"(ra), [ro2] "+v"(rb), "+{v[224:227]}"(ya), "+{v[228:231]}"(yb), "+{v[232:233]}"(yc), CLK_OUT STOPS
                : C_W, [idx] "v"(idx), [fa] "v"(fa), [vw] "v"(vw), [vr] "v"(vr), [l0] "s"(mfe.l0), [l1] "s"(mfe.l1), [l2] "s"(mfe.l2),
                  [v0] "v"(mfe.v0), [v2] "v"(mfe.v2), [hot] "s"(c.hot), [mb0] "s"(mb0), [mb1] "s"(mb1), [mb2] "s"(mb2), [force] "s"(force),
                  [ys] "v"(yo | last), [ys2] "v"(yo2 | last), [yb0] "s"(yb0), [yb1] "s"(yb1), [yb2] "s"(yb2),
                  [big] "s"(c.big), TRY_IN, [rb0] "s"(rb0), [rb1] "s"(rb1), [rb2] "s"(rb2), [zb0] "s"(zb0), [zb1] "s"(zb1), [zb2] "s"(zb2), CLK_IN
                : "memory", "vcc", "scc", "m0", CLOBC, CLOBS);
            if (__builtin_expect(miss == 0, 1)) break;
            if ((m & 3) == 3 && !image_incomplete(fa)) force = ~0ull;
            if (spin(c, m)) force = ~0ull;
            za = zb = ra = rb = OOB;
        }
        TS(4);
        wait_vm<3>();
        TS(5);
        flag(c, s, F_C + 32 * h + w);
        if (w == 0) { clk_next(c, k, TPv(s)); TSAVE(s, n); }
        sg = sg == 2 ? 0 : sg + 1;
    }
    {
        const u32 zb = hn(c, h, NSEG - 1) + y2(10 * w);
        st16(ya, c.big, y2o, zb); st16(yb, c.big, y2o, zb + 1024); st8(yc, c.big, y2o2, zb + 2048);
    }
    wait_vm<0>();
    flag(c, NSEG, F_C + 32 * h + w);
    if (w == 0) DUMP(32 + h);
}

DI void role_B(Ctx& c, const Args& a, u32 u) {
    const u32 lane = c.lane, w = c.wave;
    constexpr u32 LB_T = 131072;
    auto quiet = [&]() {
        const u32 t = rfl(*(LAS u32*)LB_T) + QUIETv;
        while ((u32)(t - rt32() - 1) < 256u) __builtin_amdgcn_s_sleep(1);
    };
    if (w >= 2) touch(c, L_GREET2, lane ? OOB : us(c, u, 0), c.big); else touch(c, L_GREET2, lane < 2 ? lane << 21 : OOB, c.hot);
    greet(c);
    greeted(c, L_GREET2);
    lw4(LB_T, 0u);
    bar();
    get_par(c, L_GREET2);
    done(c);
    if (w >= 2) {
        u32 next = 0, known = 0;
        auto poll = [&]() {
            fence_c();
            const v4u f = ld16(c.flg, F_VU * 4 + lane * 16, 0);
            u32 m = min(min(f[0], f[1]), min(f[2], f[3]));
            m = 2 * m + (lane >> 5);
            sfor<6>([&](auto I) { m = min(m, (u32)__shfl_xor((int)m, 1 << CI(I))); });
            known = rfl(m);
        };
        auto fetch = [&]() {
            const u32 dst = (next & 3) * 32768 + (w & 1) * 16384, src = us(c, u, next) + (w & 1) * 16384;
            sfor<16>([&](auto I) { dma16c(dst + CI(I) * 1024, lane * 16, c.big, src + CI(I) * 1024); });
            ++next;
        };
        {
            const u32 fo = lane < 16 ? (F_VU + ((w & 1) * 16 + lane) * 4 + u / 10) * 4 : OOB, src = us(c, u, 0) + (w & 1) * 16384;
            u32 got = 0, n = 0;
            while (got != 0xffffu && !spin(c, n)) {
                fence_c();
                u32 m = (u32)__builtin_amdgcn_ballot_w64(ld4(c.flg, fo, 0) != 0) & ~got;
                got |= m;
                for (; m; m &= m - 1) { const u32 i = rfl(__builtin_ctz(m) * 1024u); dma16c((w & 1) * 16384 + i, lane * 16, c.big, src + i); }
            }
            next = 1;
        }
        for (u32 s = 0; s < NSEG; ++s) {
            u32 n = 0;
            while (next <= s && s + 1 < NSEG) {
                if (known > next) { fetch(); continue; }
                poll();
                if (known > next) continue;
                if (spin(c, n)) break;
                __builtin_amdgcn_s_sleep(2);
            }
            if (s && next <= s + 2 && next + 1 < NSEG) {
                if (s > 1) quiet();
                if (known <= next) poll();
                if (s == 1) while (known <= next && rfl(*(volatile LAS u32*)LB_T) == 0 && !spin(c, n)) poll();
                if (known > next) fetch();
                if (known < NSEG - 1) poll();
            }
            wait_vm<0>();
            bar();
        }
        return;
    }
    const u32 mline = (u >> 5) * 16384 + (u & 31) * 256;
    const u32 l16 = lane < 16 ? lane * 16 : OOB;
    if (w == 1) {
        u32 sg = 0;
        for (u32 s = 0; s < NSEG; ++s) {
            if (s > 1) {
                quiet();
                st16(v4u{SENT, SENT, SENT, SENT}, c.hot, l16, mem_slot(c, sg) + mline);
                wait_vm<0>();
                sg = sg == 2 ? 0 : sg + 1;
            }
            bar();
        }
    } else {
        {
            const rsrc_t rm = mkrs(a.mem0, 8 * 1280);
            const u32 t = 2 * (lane >> 2) + (lane & 1), hf = (lane >> 1) & 1;
            v4u v = __builtin_bit_cast(v4u, __builtin_amdgcn_raw_buffer_load_b128(rm, lane < 16 ? t * 1280 + hf * 16 : OOB, u * 32, 0));
            st16(v4u{canon(v[0]), canon(v[1]), canon(v[2]), canon(v[3])}, c.hot, l16, spin_s(mem_slot(c, 0) + mline));
        }
        EV(2);
        bar();
        EV(3);
        u32 sg = 0;
        Clk k;
        clk_init(c, k, TFv(0));
        const u32 vo = lane * 16, idx = sp_idx(lane);
        const u32 so = (4 * (lane >> 4) + 2 * ((lane >> 3) & 1) + (lane & 1)) * 16 + (lane & 6) * 2;
        const u32 sel = (lane & 1) ? 0x03020706u : 0x05040100u;
        TSDECL;
        for (u32 s = 0; s + 1 < NSEG; ++s) {
            v8u z[16];
            sfor<16>([&](auto I) {
                const v4u lo = lr16((s & 3) * 32768 + CI(I) * 2048 + vo), hi = lr16((s & 3) * 32768 + CI(I) * 2048 + 1024 + vo);
                z[CI(I)] = v8u{lo[0], lo[1], lo[2], lo[3], hi[0], hi[1], hi[2], hi[3]};
            });
            sfor<16>([&](auto I) { pina(z[CI(I)]); });
            const u32 nx = sg == 2 ? 0 : sg + 1;
            const u32 y0 = y_slot(c, sg) + u * 16384, y1 = y0 + 4096, p0 = p_slot(c, sg);
            const u32 p1 = p0 + 4096, p2 = p0 + 16384, p3 = p0 + 20480, p4 = p0 + 32768;
            const u32 mo = mem_slot(c, nx) + mline;
            const u32 ex = rfl(TPv(s) == TPv(0) ? BEX : BEX2);
            u32 n = 0, m = 0;
            for (;;) {
                u32 miss;
                const u32 chk = rfl((m + 1) & ~m & 4), lim = rfl(s ? n + 3 : n);
                if (s == 0 && m == 0) gate(c, lane < 32 ? p4 + (lane >> 3) * 1024 + ((lane >> 1) & 3) * 256 + (lane & 1) * 8 : y0 + (lane - 32) * 256, 0);
                u32 tm = clk_mode(c, k, s != 0), late = 0;
                asm volatile(B_BODY
                    : TRY_OUT, [late] "+s"(late), CLK_OUT STOPS
                    : B_Z, [idx] "v"(idx), [vo] "v"(vo), [hot] "s"(c.hot), [y0] "s"(y0), [y1] "s"(y1), [p0] "s"(p0), [p1] "s"(p1), [p2] "s"(p2), [p3] "s"(p3), [p4] "s"(p4),
                      [sel] "v"(sel), [so] "v"(so), [mo] "s"(mo), [chk] "s"(chk), [ex] "s"(ex), TRY_IN, CLK_IN
                    : "memory", "vcc", "scc", CLOB, CLOBS);
                if (__builtin_expect(miss == 0, 1)) break;
                if (spin(c, m)) break;
            }
            TSAVE(s, n);
            sg = nx;
            if (s == 0) {
                const u32 t = rt32() + GAP - TPv(0);
                while ((int)(k.F - t) < 0) k.F += TQv;
                k.F = rfl(k.F);
            }
            lw4(LB_T, k.F + TPv(s) - TFv(s));
            clk_next(c, k, TPv(s) - TFv(s) + TFv(s + 1));
            bar();
        }
        DUMP(64 + u);
    }
}

constexpr u32 L_V = 80 * 1024;
DI u32 xslot(u32 slot) { return slot == 0 ? 120u * 1024 : (slot - 1) * 40960; }
DI u32 x_src(const Ctx& c) { return (c.wave * 8 + (c.lane >> 3)) * 1280 + ((c.lane ^ (c.lane >> 3)) & 7) * 16; }
DI void stage_x1(const Ctx& c, rsrc_t rx, u32 vo, u32 s, u32 slot, int i) { dma16(xslot(slot) + c.wave * 10240 + i * 1024, vo, rx, s * 40960 + i * 128); }
DI void stage_x(const Ctx& c, rsrc_t rx, u32 vo, u32 s, u32 slot) { sfor<10>([&](auto I) { stage_x1(c, rx, vo, s, slot, CI(I)); }); }
DI u32 x_frag(const Ctx& c) { const u32 r = c.lane & 7; return ((c.lane >> 3) & 1) * 10240 + r * 128 + (((c.lane >> 4) ^ r) * 16); }
template <int KS, int TT> DI v4u x_read(u32 x0, u32 x1) { return lr16(((KS & 1) ? x1 : x0) + TT * 20480 + (KS >> 1) * 1024); }
template <bool Z, bool AA, bool BA> DI void mfx(f32x4& acc, const v4u& a, const v4u& b) {
    if constexpr (Z) {
        if constexpr (AA) asm volatile("v_mfma_f32_16x16x32_bf16 %0, %1, %2, 0" : "=&v"(acc) : "a"(a), "v"(b) : "memory");
        else if constexpr (BA) asm volatile("v_mfma_f32_16x16x32_bf16 %0, %1, %2, 0" : "=&v"(acc) : "v"(a), "a"(b) : "memory");
        else asm volatile("v_mfma_f32_16x16x32_bf16 %0, %1, %2, 0" : "=&v"(acc) : "v"(a), "v"(b) : "memory");
    } else {
        if constexpr (AA) asm volatile("v_mfma_f32_16x16x32_bf16 %0, %1, %2, %0" : "+v"(acc) : "a"(a), "v"(b) : "memory");
        else if constexpr (BA) asm volatile("v_mfma_f32_16x16x32_bf16 %0, %1, %2, %0" : "+v"(acc) : "v"(a), "a"(b) : "memory");
        else asm volatile("v_mfma_f32_16x16x32_bf16 %0, %1, %2, %0" : "+v"(acc) : "v"(a), "v"(b) : "memory");
    }
}
template <bool Z, bool AA, bool BA> DI void mfn(f32x4& acc, const v4u& a, const v4u& b) {
    if constexpr (Z) acc = f32x4{};
    if constexpr (AA) asm volatile("s_nop 1\n\tv_mfma_f32_16x16x32_bf16 %0, %1, %2, %0" : "+v"(acc) : "a"(a), "v"(b) : "memory");
    else if constexpr (BA) asm volatile("s_nop 1\n\tv_mfma_f32_16x16x32_bf16 %0, %1, %2, %0" : "+v"(acc) : "v"(a), "a"(b) : "memory");
    else asm volatile("s_nop 1\n\tv_mfma_f32_16x16x32_bf16 %0, %1, %2, %0" : "+v"(acc) : "v"(a), "v"(b) : "memory");
}
DI v4u cvt8(const f32x4& lo, const f32x4& hi) { return v4u{vcvt(lo[0], lo[1]), vcvt(lo[2], lo[3]), vcvt(hi[0], hi[1]), vcvt(hi[2], hi[3])}; }
DI void role_QK(Ctx& c, const Args& a, u32 h, u32 pi) {
    const u32 lane = c.lane, w = c.wave;
    const rsrc_t rx = mkrs(a.x, 2048u * 1280);
    const u32 xv = x_src(c), xf = x_frag(c);
    greet_ask(c);
    touch(c, L_GREET, lane ? OOB : hf(c, h, pi), c.big);
    stage_x(c, rx, xv, pi, 0);
    v4u W[20][4];
    {
        v2u t[2][2];
        f32x4 acc[4][2];
        v4u xr[2];
        const u32 x0 = xslot(0) + xf, x1 = x0 ^ 64;
        const rsrc_t rq = mkrs(a.wq, 640u * 8192), rk = mkrs(a.wk, 640u * 8192);
        const u32 vo = row_f2(lane) * 8192 + (h * 128 + w * 32) * 2 + dma_chunk(lane);
        load_weights<28>(c,
            [&](auto B, u32 dst) { constexpr int b = CI(B), m = (b >> 1) & 1, ks = b >> 2, tt = b & 1; dma16(dst, vo, m ? rq : rk, (32 * ks + 4 * tt) * 8192u); },
            [&](auto B, v2u lo, v2u hi) {
                constexpr int b = CI(B), m = (b >> 1) & 1, ks = b >> 2, tt = b & 1;
                if constexpr ((b & 3) == 0) { xr[0] = x_read<ks, 0>(x0, x1); xr[1] = x_read<ks, 1>(x0, x1); }
                t[0][tt] = lo; t[1][tt] = hi;
                if constexpr (tt == 1) {
                    v4u w0 = cat4(t[0][0], t[0][1]), w1 = cat4(t[1][0], t[1][1]);
                    if constexpr (ks < 16) { asm volatile("" : "+a"(w0)); asm volatile("" : "+a"(w1)); }
                    else { asm volatile("" : "+v"(w0)); asm volatile("" : "+v"(w1)); }
                    W[ks][2 * m] = w0; W[ks][2 * m + 1] = w1;
                    sfor<4>([&](auto T) { mfn<ks == 0, (ks < 16), false>(acc[2 * m + (CI(T) >> 1)][CI(T) & 1], W[ks][2 * m + (CI(T) >> 1)], xr[CI(T) & 1]); });
                }
            }, [&](auto CH) { greet_on<CI(CH)>(c, L_GREET); if constexpr (CI(CH) == 0) bar(); });
        EV(1);
        nop<7>(); nop<7>();
        sfor<4>([&](auto I) {
            constexpr int m = CI(I) >> 1, tt = CI(I) & 1;
            __builtin_amdgcn_raw_buffer_store_b128(cvt8(acc[2 * m][tt], acc[2 * m + 1][tt]), c.big, lane * 16 + w * 1024, hf(c, h, pi) + m * H_QX + tt * 4096, BGA);
        });
    }
    greeted_late(c, L_GREET);
    bar();
    wait_vm<0>(); flag(c, 1, F_QK + (pi * 32 + h) * 4 + w); done(c); EV(3); stage_x(c, rx, xv, pi + 2, 1); stage_x(c, rx, xv, pi + 4, 2);
    u32 slot = 1;
    for (u32 n = 1; n < NSEG / 2; ++n) {
        const u32 s = 2 * n + pi;
        if (n <= 1) wait_vm<10>(); else wait_vm<15>();
        bar();
        f32x4 acc[4][2];
        v4u xr[3][2];
        const u32 x0 = xslot(slot) + xf, x1 = x0 ^ 64, sn = n + 2 < NSEG / 2 ? s + 4 : s, sl = slot == 0 ? 2 : slot - 1, xn = xv;
        sfor<2>([&](auto K) { xr[CI(K)][0] = x_read<CI(K), 0>(x0, x1); xr[CI(K)][1] = x_read<CI(K), 1>(x0, x1); });
        sfor<20>([&](auto K) {
            constexpr int ks = CI(K);
            if constexpr (ks + 2 < 20) { xr[(ks + 2) % 3][0] = x_read<ks + 2, 0>(x0, x1); xr[(ks + 2) % 3][1] = x_read<ks + 2, 1>(x0, x1); }
            sfor<8>([&](auto T) {
                mfx<ks == 0, (ks < 16), false>(acc[CI(T) >> 1][CI(T) & 1], W[ks][CI(T) >> 1], xr[ks % 3][CI(T) & 1]);
                if constexpr (CI(T) == 0 && (ks & 1) == 0) stage_x1(c, rx, xn, sn, sl, ks >> 1);
            });
            if constexpr (ks == 9) { wait_vm<5>(); flag(c, n, F_QK + (pi * 32 + h) * 4 + w); }
        });
        nop<3>();
        sfor<4>([&](auto I) {
            constexpr int m = CI(I) >> 1, tt = CI(I) & 1;
            __builtin_amdgcn_raw_buffer_store_b128(cvt8(acc[2 * m][tt], acc[2 * m + 1][tt]), c.big, lane * 16 + w * 1024, hf(c, h, s) + m * H_QX + tt * 4096, BGA);
        });
        if (n == 1) { wait_vm<0>(); flag(c, 2, F_QK + (pi * 32 + h) * 4 + w); }
        slot = slot == 2 ? 0 : slot + 1;
    }
    wait_vm<0>();
    flag(c, NSEG / 2, F_QK + (pi * 32 + h) * 4 + w);
}
DI void role_VU(Ctx& c, const Args& a, u32 h, u32 pi) {
    const u32 lane = c.lane, w = c.wave;
    const rsrc_t rx = mkrs(a.x, 2048u * 1280);
    const u32 xv = x_src(c), xf = x_frag(c);
    greet_ask(c);
    touch(c, L_GREET, lane < 10 ? us(c, 10 * w + lane, pi) : OOB, c.big);
    stage_x(c, rx, xv, pi, 0);
    v4u Wv[20][2], Wo[4][10];
    const u32 uo = h * 1024 + lane * 16;
    {
        v2u t[2][2];
        f32x4 acc[2][2], u0[10][2];
        v4u xr[2], vf[2][4], o[10];
        const u32 x0 = xslot(0) + xf, x1 = x0 ^ 64;
        const rsrc_t rv = mkrs(a.wv, 640u * 8192), ro = mkrs(a.wo, 4096u * 1280);
        const u32 vo = row_f2(lane) * 8192 + (h * 128 + w * 32) * 2 + dma_chunk(lane);
        const u32 vo2 = (h * 128 + row_f3(lane)) * 1280 + w * 320 + dma_chunk(lane);
        load_weights<28>(c,
            [&](auto B, u32 dst) {
                constexpr int b = CI(B), tt = b & 1;
                if constexpr (b < 40) dma16(dst, vo, rv, (32 * (b >> 1) + 4 * tt) * 8192u);
                else { constexpr int u = (b - 40) >> 1, es = u / 5, cp = u % 5; dma16(dst, vo2, ro, (32 * es + 16 * tt) * 1280u + cp * 64); }
            },
            [&](auto B, v2u lo, v2u hi) {
                constexpr int b = CI(B), tt = b & 1;
                if constexpr (b < 40 && tt == 0) { xr[0] = x_read<(b >> 1), 0>(x0, x1); xr[1] = x_read<(b >> 1), 1>(x0, x1); }
                if constexpr (b == 40) {
                    nop<7>(); nop<7>();
                    bar();
                    sfor<2>([&](auto T) { lw16(xslot(0) + CI(T) * 4096 + w * 1024 + lane * 16, cvt8(acc[0][CI(T)], acc[1][CI(T)])); });
                    bar();
                    sfor<8>([&](auto I) { vf[CI(I) & 1][CI(I) >> 1] = lr16(xslot(0) + (CI(I) & 1) * 4096 + (CI(I) >> 1) * 1024 + lane * 16); });
                }
                t[0][tt] = lo; t[1][tt] = hi;
                if constexpr (tt == 1) {
                    v4u w0 = cat4(t[0][0], t[0][1]), w1 = cat4(t[1][0], t[1][1]);
                    if constexpr (b < 64) { asm volatile("" : "+a"(w0)); asm volatile("" : "+a"(w1)); }
                    else { asm volatile("" : "+v"(w0)); asm volatile("" : "+v"(w1)); }
                    if constexpr (b < 40) {
                        Wv[b >> 1][0] = w0; Wv[b >> 1][1] = w1;
                        sfor<4>([&](auto T) { mfn<b == 1, true, false>(acc[CI(T) >> 1][CI(T) & 1], Wv[b >> 1][CI(T) >> 1], xr[CI(T) & 1]); });
                    } else {
                        constexpr int u = (b - 40) >> 1, es = u / 5, cp = u % 5;
                        Wo[es][2 * cp] = w0; Wo[es][2 * cp + 1] = w1;
                        sfor<4>([&](auto T) { constexpr int ct = 2 * cp + (CI(T) >> 1); mfn<es == 0, false, (b < 64)>(u0[ct][CI(T) & 1], vf[CI(T) & 1][es], Wo[es][ct]); });
                        if constexpr (es == 3) {
                            nop<7>(); nop<7>();
                            sfor<2>([&](auto T) {
                                constexpr int ct = 2 * cp + CI(T);
                                o[ct] = v4u{vcvt(u0[ct][0][0], u0[ct][0][1]), vcvt(u0[ct][1][0], u0[ct][1][1]), vcvt(u0[ct][0][2], u0[ct][0][3]), vcvt(u0[ct][1][2], u0[ct][1][3])};
                            });
                        }
                    }
                }
            }, [&](auto CH) { greet_on<CI(CH)>(c, L_GREET); if constexpr (CI(CH) == 0) bar(); });
        EV(1);
        sfor<10>([&](auto T) { __builtin_amdgcn_raw_buffer_store_b128(o[CI(T)], c.big, uo, us(c, 10 * w + CI(T), pi), BGA); });
    }
    greeted_late(c, L_GREET);
    bar();
    wait_vm<0>(); flag(c, 1, F_VU + (pi * 32 + h) * 4 + w); done(c); EV(3); stage_x(c, rx, xv, pi + 2, 1); stage_x(c, rx, xv, pi + 4, 2);
    u32 slot = 1;
    for (u32 n = 1; n < NSEG / 2; ++n) {
        const u32 s = 2 * n + pi;
        if (n <= 1) wait_vm<10>(); else wait_vm<21>();
        bar();
        f32x4 acc[2][2];
        v4u xr[4][2];
        const u32 x0 = xslot(slot) + xf, x1 = x0 ^ 64, sn = n + 2 < NSEG / 2 ? s + 4 : s, sl = slot == 0 ? 2 : slot - 1, xn = xv;
        sfor<3>([&](auto K) { xr[CI(K)][0] = x_read<CI(K), 0>(x0, x1); xr[CI(K)][1] = x_read<CI(K), 1>(x0, x1); });
        sfor<20>([&](auto K) {
            constexpr int ks = CI(K);
            if constexpr (ks + 3 < 20) { xr[(ks + 3) % 4][0] = x_read<ks + 3, 0>(x0, x1); xr[(ks + 3) % 4][1] = x_read<ks + 3, 1>(x0, x1); }
            sfor<4>([&](auto T) {
                mfx<ks == 0, true, false>(acc[CI(T) >> 1][CI(T) & 1], Wv[ks][CI(T) >> 1], xr[ks % 4][CI(T) & 1]);
                if constexpr (CI(T) == 0 && (ks & 1) == 0) stage_x1(c, rx, xn, sn, sl, ks >> 1);
            });
        });
        const u32 lv = L_V + (n & 1) * 8192;
        wait_vm<10>();
        flag(c, n, F_VU + (pi * 32 + h) * 4 + w);
        nop<5>();
        sfor<2>([&](auto T) { constexpr int tt = CI(T); lw16(lv + tt * 4096 + w * 1024 + lane * 16, cvt8(acc[0][tt], acc[1][tt])); });
        bar();
        v4u vf[2][4];
        sfor<8>([&](auto I) { vf[CI(I) & 1][CI(I) >> 1] = lr16(lv + (CI(I) & 1) * 4096 + (CI(I) >> 1) * 1024 + lane * 16); });
        f32x4 u[2][2];
        v4u o;
        sfor<11>([&](auto T) {
            constexpr int ct = CI(T), p = ct & 1, q = p ^ 1;
            sfor<4>([&](auto E) {
                constexpr int e = CI(E);
                constexpr bool ag = (40 + 2 * (5 * e + (ct < 10 ? ct : 0) / 2)) < 64;
                if constexpr (ct < 10) { mfx<e == 0, false, ag>(u[p][0], vf[0][e], Wo[e][ct]); mfx<e == 0, false, ag>(u[p][1], vf[1][e], Wo[e][ct]); }
                else if constexpr (e == 0) nop<7>();
                if constexpr (ct > 0 && e == 1) { o[0] = vcvt(u[q][0][0], u[q][0][1]); o[2] = vcvt(u[q][0][2], u[q][0][3]); }
                if constexpr (ct > 0 && e == 2) { o[1] = vcvt(u[q][1][0], u[q][1][1]); o[3] = vcvt(u[q][1][2], u[q][1][3]); }
                if constexpr (ct > 0 && e == 3) __builtin_amdgcn_raw_buffer_store_b128(o, c.big, uo, us(c, 10 * w + ct - 1, s), BGA);
            });
        });
        if (n == 1) { wait_vm<0>(); flag(c, 2, F_VU + (pi * 32 + h) * 4 + w); }
        slot = slot == 2 ? 0 : slot + 1;
    }
    wait_vm<0>();
    flag(c, NSEG / 2, F_VU + (pi * 32 + h) * 4 + w);
}
#define FSLEEP 2
#define FD 1
#define FP1 300
#define FP2 250
#define FP3 60
constexpr u32 SF = 58;
DI void role_F(Ctx& c, const Args& a, u32 id) {
    const u32 lane = c.lane, w = c.wave, g = lane >> 4, i16 = lane & 15;
    const rsrc_t ro = mkrs(a.out, 2048u * 1280);
    bar();
    EV(4);
    if (w == 0) { wait_flags4(c, F_VU, NSEG / 2); wait_flags4(c, F_VU + 128, NSEG / 2); }
    EV(5);
    u32 par = 0;
    v4u za[10][2], pb[10][2];
    auto ready = [&](u32 s) {
        if (w == 0) {
            u32 n = 0;
            for (;;) {
                fence_c();
                const v4u f4 = ld16(c.flg, F_A * 4 + lane * 128, 0);
                const u32 f = lane < 32 ? f4[0] : min(min(f4[0], f4[1]), min(f4[2], f4[3]));
                if (__builtin_amdgcn_ballot_w64(f <= s) == 0) break;
                if (spin(c, n)) break;
                if (s < SF || __builtin_amdgcn_ballot_w64(f + 2 <= s) != 0) __builtin_amdgcn_s_sleep(16); else __builtin_amdgcn_s_sleep(FSLEEP);
            }
        }
        bar();
    };
    auto ld_p = [&](auto N, u32 s, u32 q0) {
        sfor<8>([&](auto K) { sfor<CI(N)>([&](auto Q) { pb[CI(K)][CI(Q)] = ld16(c.big, lane * 16 + CI(Q) * 1024, hn(c, 8 * w + CI(K), s) + H_PU + q0 * 1024); }); });
        sfor<2>([&](auto K) { sfor<CI(N)>([&](auto Q) { pb[8 + CI(K)][CI(Q)] = ld16(c.big, hn(c, 8 * w + g + 4 * CI(K), s) + H_PM + i16 * 16 + CI(Q) * 256, q0 * 256); }); });
    };
    auto ld_u = [&](auto N, u32 s, u32 u0) {
        sfor<8>([&](auto K) { sfor<CI(N)>([&](auto U) { za[CI(K)][CI(U)] = ld16(c.big, lane * 16 + CI(K) * 1024, us(c, u0 + CI(U), s) + w * 8192); }); });
    };
    auto ld_y = [&](auto N, u32 s, u32 u0) {
        sfor<2>([&](auto K) { sfor<CI(N)>([&](auto U) { za[8 + CI(K)][CI(U)] = ld16(c.big, hn(c, 8 * w + g + 4 * CI(K), s) + i16 * 16 + CI(U) * 256, y2(u0)); }); });
    };
    auto ld_k = [&](auto K, u32 s, u32 u0) {
        sfor<2>([&](auto U) {
            if constexpr (CI(K) < 8) za[CI(K)][CI(U)] = ld16(c.big, lane * 16 + CI(K) * 1024, us(c, u0 + CI(U), s) + w * 8192);
            else if constexpr (CI(K) < 10) za[CI(K)][CI(U)] = ld16(c.big, hn(c, 8 * w + g + 4 * (CI(K) - 8), s) + i16 * 16 + CI(U) * 256, y2(u0));
        });
    };
    auto fin = [&](auto N, auto D, u32 s, u32 q0, u32 u0) {
        constexpr int n = CI(N), d = CI(D);
        sfor<d>([&](auto K) { ld_k(K, s, u0); });
        __builtin_amdgcn_sched_barrier(0);
        f32x4 acc[n][n] = {};
        sfor<10>([&](auto K) {
            sfor<n * n>([&](auto I) { constexpr int ct = CI(I) % n, qt = CI(I) / n; acc[qt][ct] = mfma(za[CI(K)][ct], pb[CI(K)][qt], acc[qt][ct]); });
            if constexpr (d > 0) { ld_k(std::integral_constant<int, CI(K) + d>{}, s, u0); __builtin_amdgcn_sched_barrier(0); }
        });
        const u32 lb = par * 16384;
        sfor<n * n>([&](auto I) { lw16(lb + (w * 4 + CI(I)) * 1024 + lane * 16, __builtin_bit_cast(v4u, acc[CI(I) / n][CI(I) % n])); });
        bar();
        if (w < n) {
            const u32 la = lb + (w * n + (g >> 1)) * 1024 + ((g & 1) * 32 + i16) * 16;
            f32x4 r0 = {}, r1 = {};
            sfor<4>([&](auto V) {
                r0 += __builtin_bit_cast(f32x4, lr16(la + CI(V) * 4096));
                r1 += __builtin_bit_cast(f32x4, lr16(la + CI(V) * 4096 + 256));
            });
            const v4u o = {pk(r0[0], r0[1]), pk(r0[2], r0[3]), pk(r1[0], r1[1]), pk(r1[2], r1[3])};
            __builtin_amdgcn_raw_buffer_store_b128(o, ro, g < 2 * n ? (s * 32 + (q0 + w) * 16 + i16) * 1280 + u0 * 32 + g * 16 : OOB, 0, 0);
        }
        par ^= 1;
    };
    constexpr std::integral_constant<int, 1> one;
    constexpr std::integral_constant<int, 2> two;
    constexpr std::integral_constant<int, 0> all;
    constexpr std::integral_constant<int, FD> few;
    auto until = [&](u32 t0, u32 d) { while (rt32() - t0 < d) __builtin_amdgcn_s_sleep(4); };
    auto warm = [&](u32 s) { const u32 v = ld4(c.big, lane < 2 ? hn(c, 8 * w + 7 * lane, s) + H_PU : OOB, 0); asm volatile("" ::"v"(v)); };
    for (u32 task = id; task < 4 * SF; task += 128) {
        const u32 s = task >> 2, pace = task < 128 ? FP1 : s < 50 ? FP2 : FP3;
        ready(s);
        u32 t0 = rt32();
        if (task < 128) { until(t0, id * FP1 >> 7); t0 = rt32(); }
        ld_p(two, s, 0);
        for (u32 i = 0; i < 5; ++i) {
            const u32 u0 = (task & 3) * 10 + 2 * i;
            until(t0, i * pace);
            fence_c();
            if (s < 50) fin(two, few, s, 0, u0);
            else { ld_u(two, s, u0); ld_y(two, s, u0); fin(two, all, s, 0, u0); }
        }
    }
    const u32 j = (id + 24) & 127;
    if (j < (NSEG - 1 - SF) * 20) {
        const u32 s = SF + j / 20, u0 = j % 20 * 2;
        ld_u(two, s, u0); warm(s);
        ready(s);
        ld_p(two, s, 0); ld_y(two, s, u0);
        fin(two, all, s, 0, u0);
    }
    if (j < 80) {
        const u32 s = NSEG - 1, u0 = j >> 1, q0 = j & 1;
        ld_u(one, s, u0); warm(s);
        ready(s);
        ld_p(one, s, q0); ld_y(one, s, u0);
        fin(one, all, s, q0, u0);
    }
}

DI u32 probe_off(u32 page, u32 i) { return page * (2u << 20) + (100u + (i >> 1)) * 16384u + (i & 1) * 8192u + (i >> 1) * 1152u; }
DI char* hot_base(char* ws) { return (char*)(((u64)ws + FLAG_BYTES + HOT_BYTES - 1) & ~(u64)(HOT_BYTES - 1)); }
DI u32 xcc_id() { return (u32)__builtin_amdgcn_s_getreg(20 | (3 << 11)) & 0xfu; }

DI u32 probe1(const Ctx& c, u32 off) {
    const u32 t0 = mt32();
    wait_lgkm();
    u32 v = ld4(c.hot, off, 0);
    asm volatile("s_waitcnt vmcnt(0)" : "+v"(v)::"memory");
    return mt32() - t0;
}
DI void probe(Ctx& c, u32 page) {
    u32 m0 = 0xffff, m1 = 0xffff, i = 0;
    for (; i < 24; ++i) {
        const u32 t0 = probe1(c, probe_off(page, 2 * (i & 7))), t1 = probe1(c, probe_off(page, 2 * (i & 7) + 1));
        if (i) { m0 = min(m0, t0); m1 = min(m1, t1); }
        const u32 df = m0 > m1 ? m0 - m1 : m1 - m0;
        if (i >= 7 || (i >= 3 && df > 160)) lw4(8192 + page * 4, m1 < m0 ? 3u : 2u);
        if (rfl(*(volatile LAS u32*)8200)) break;
    }
}
#define XD 0
DI void role_init(Ctx& c, const Args& a, u32 k) {
    const u32 lane = c.lane, w = c.wave, t0 = rt32();
    if (k == 0) {
        for (u32 o = F_ZERO * 4 + w * 1024; o < 16384; o += 4096) st16(v4u{0, 0, 0, 0}, c.flg, lane * 16, o);
        if (w == 1 || w == 2) { st4(0u, c.hot, lane < 16 ? probe_off(w - 1, lane) : OOB, 0); lw4(8192 + (w - 1) * 4, 0u); lw4(8200, 0u); }
        wait_vm<0>();
        bar();
        if (w == 1 || w == 2) probe(c, w - 1);
        if (w) return;
        flag(c, 1u, F_DONE + c.id);
        u32 n = 0, mask = lane < 58 ? OOB : 0u, par = 0, fixed = 0;
        for (;;) {
            fence_c();
            const v4u ch = ld16(c.flg, F_CH * 4 + lane * 16, 0), d = ld16(c.flg, F_DONE * 4 + lane * 16, 0);
            const u32 p0 = rfl(*(volatile LAS u32*)8192), p1 = rfl(*(volatile LAS u32*)8196);
            const u64 open = __builtin_amdgcn_ballot_w64((d[0] & d[1] & d[2] & d[3]) == 0);
            if (open == 0 || spin(c, n)) break;
            if (!fixed && (open >> 58) == 0 && p0 && p1) { fixed = 1; mask = 0; par = (p0 & 1) | (p1 & 1) << 1; lw4(8200, 1u); }
            st16((ch & ~3u) | par, c.flg, (F_RS * 4 + lane * 16) | mask, 0);
        }
        return;
    }
    greet(c);
    for (u32 i = (k - 1) * 4 + w; i < 135 * 16; i += 92) st16(v4u{SENT, SENT, SENT, SENT}, c.hot, lane * 16, (i < 95 * 16 ? i : i + 33 * 16) * 1024);
    wait_vm<0>();
    bar();
    greeted(c, L_GREET2);
    done(c);
    const rsrc_t rx = mkrs(a.x, 2048u * 1280);
    while (rt32() - t0 < XD) __builtin_amdgcn_s_sleep(8);
    for (u32 i = (k - 1) * 4 + w; i < 2560; i += 92) dma16(w * 1024, lane * 16, rx, i * 1024);
    wait_vm<0>();
}

DI void code_ahead(const Ctx& c, u32 lds, u32 r) {
    u64 pc;
    u32 len;
    asm volatile("s_getpc_b64 %0\n1:\n\ts_mov_b32 %1, 2f-1b\n\t.subsection 2\n2:\n\t.subsection 0" : "=s"(pc), "=s"(len));
    dma16(lds + c.wave * 1024, c.lane * 16 + (r * 4 + c.wave) * 1024, mkrs((const void*)pc, len & ~15u), 0);
}
__global__ void __launch_bounds__(256, 1) k_main(Args a) {
    extern __shared__ __attribute__((aligned(16))) char lds_[];
    Ctx c;
    c.lane = threadIdx.x & 63; c.wave = rfl(threadIdx.x >> 6); c.dead = 0; c.l0 = c.lane ? OOB : 0u;
    char* hot = hot_base(a.ws);
    c.flg = mkrs(a.ws, FLAG_BYTES); c.hot = mkrs(hot, HOT_BYTES); c.big = mkrs(hot + HOT_BYTES, BIG_BYTES);
    const u32 role = (blockIdx.x & 7) * 32 + (blockIdx.x >> 3);
    const u32 x = role >> 5, r = role & 31;
    c.role = role;
    code_ahead(c, x < 4 ? 146u * 1024 : 114u * 1024, r);
    c.id = x >= 4 ? role - 24 : r < 16 ? (r >> 3) * 32 + 8 * x + (r & 7) : r < 26 ? 48 + 10 * x + r : 206 + 6 * x + r;
    EV(1);
    if (x < 4) {
        if (r < 8) role_A(c, a, 8 * x + r);
        else if (r < 16) role_C(c, a, 8 * x + r - 8);
        else if (r < 26) role_B(c, a, 10 * x + r - 16);
        else role_init(c, a, c.id - 232);
    } else {
        const u32 h = 8 * (x - 4) + (r & 7), kind = r >> 3;
        if (kind < 2) role_QK(c, a, h, kind); else role_VU(c, a, h, kind - 2);
        role_F(c, a, role - 128);
    }
    EV(7);
    (void)lds_;
}

extern "C" void kernel_launch(void* const* d_in, const int* in_sizes, int n_in, void* d_out, int out_size, void* d_ws, size_t ws_size, hipStream_t stream) {
    (void)in_sizes; (void)n_in; (void)out_size; (void)ws_size;
    Args a;
    a.x = (const u16*)d_in[0]; a.mem0 = (const u16*)d_in[1]; a.wq = (const u16*)d_in[2]; a.wk = (const u16*)d_in[3];
    a.wv = (const u16*)d_in[4]; a.wo = (const u16*)d_in[5]; a.out = (u16*)d_out; a.ws = (char*)d_ws;
    (void)hipFuncSetAttribute((const void*)k_main, hipFuncAttributeMaxDynamicSharedMemorySize, LDS_BYTES);
    k_main<<<256, 256, LDS_BYTES, stream>>>(a);
}
```
